```python
import torch

def _softmax(x, axis=-1):
    return torch.softmax(x, dim=axis)
def _gelu(x):
    return torch.nn.functional.gelu(x, approximate="tanh")
def _silu(x):
    return torch.nn.functional.silu(x)
def _sigmoid(x):
    return torch.sigmoid(x)
def _layernorm(x, axis=-1, eps=1e-5):
    m = x.mean(dim=axis, keepdim=True)
    v = x.var(dim=axis, keepdim=True, unbiased=False)
    return (x - m) / torch.sqrt(v + eps)
def _scatter_add(tgt, idx, src, axis=0):
    return tgt.clone().index_add_(axis, idx, src)
def _segment_sum(src, seg, n):
    out = torch.zeros((n,) + src.shape[1:], dtype=src.dtype)
    return out.index_add_(0, seg, src)
def _pad(x, pw):
    flat = tuple(v for pair in reversed(pw) for v in pair)
    return torch.nn.functional.pad(x, flat)

kernel_name = 'parallel_sum__sliding_block_cached__glu_gated_glu__decode'

def setup_inputs():

    _w = (lambda a: (lambda t: t.to(torch.bfloat16) if t.is_floating_point() else t)(torch.as_tensor(a)))
    def _A_setup():
        B, Sq, D = 1, 1, 448
        Hq, Hkv, Sk, Dh = 16, 8, 65536, 256
        assert Hq % Hkv == 0, (Hq, Hkv)
        return {
            "x": _w((lambda *s: torch.randn(*s, dtype=torch.bfloat16) * 0.05)(B, Sq, D)),
            "k_cache": _w((lambda *s: torch.randn(*s, dtype=torch.bfloat16) * 3.2)(B, Hkv, Sk, Dh)),
            "v_cache": _w((lambda *s: torch.randn(*s, dtype=torch.bfloat16) * 1.6)(B, Hkv, Sk, Dh)),
            "wq": _w((lambda *s: torch.randn(*s, dtype=torch.bfloat16) * 3.2)(D, Hq * Dh) * (D ** -0.5)),
            "wo": _w((lambda *s: torch.randn(*s, dtype=torch.bfloat16) * 1.6)(Hq * Dh, D) * ((Hq * Dh) ** -0.5)),
        }
    _A_in = _A_setup()

    def _B_setup():
        B, S, D, F = 1, 1, 448, 1792
        return {
            "x": _w((lambda *s: torch.randn(*s, dtype=torch.bfloat16) * 0.05)(B, S, D)),
            "w_gate": _w((lambda *s: torch.randn(*s, dtype=torch.bfloat16) * 0.1)(D, F)),
            "w_up": _w((lambda *s: torch.randn(*s, dtype=torch.bfloat16) * 0.05)(D, F)),
            "w_down": _w((lambda *s: torch.randn(*s, dtype=torch.bfloat16) * 0.05)(F, D)),
        }
    _B_in = _B_setup()

    return {
        "A_x": _A_in["x"],
        "A_k_cache": _A_in["k_cache"],
        "A_v_cache": _A_in["v_cache"],
        "A_wq": _A_in["wq"],
        "A_wo": _A_in["wo"],
        "B_x": _B_in["x"],
        "B_w_gate": _B_in["w_gate"],
        "B_w_up": _B_in["w_up"],
        "B_w_down": _B_in["w_down"],
    }

def reference(A_x, A_k_cache, A_v_cache, A_wq, A_wo, B_x, B_w_gate, B_w_up, B_w_down):
    def _A_ref(x, k_cache, v_cache, wq, wo):
        B, Sq, D = x.shape
        Hq, Dh = 16, 256
        Hkv, Sk = k_cache.shape[1], k_cache.shape[2]
        W = min(4096, max(256, Sk // 4))
        rep = Hq // Hkv
        q = (lambda x, *d: x.permute(*d))((x @ wq).reshape(B, Sq, Hq, Dh), 0, 2, 1, 3)
        k = (lambda x, n, axis: torch.repeat_interleave(x, n, dim=axis))(k_cache[:, :, Sk - W:, :], rep, axis=1)
        v = (lambda x, n, axis: torch.repeat_interleave(x, n, dim=axis))(v_cache[:, :, Sk - W:, :], rep, axis=1)
        scores = torch.einsum("bhqd,bhkd->bhqk", q, k) * (Dh ** -0.5)
        p = _softmax(scores, axis=-1)
        o = torch.einsum("bhqk,bhkd->bhqd", p, v)
        return (lambda x, *d: x.permute(*d))(o, 0, 2, 1, 3).reshape(B, Sq, Hq * Dh) @ wo

    def _B_ref(x, w_gate, w_up, w_down):
        g = torch.einsum("bsd,df->bsf", x, w_gate)
        u = torch.einsum("bsd,df->bsf", x, w_up)
        h = _sigmoid(g) * u
        return torch.einsum("bsf,fd->bsd", h, w_down)

    x = A_x
    xn = _layernorm(x, axis=-1)
    return x + _A_ref(xn, A_k_cache, A_v_cache, A_wq, A_wo) + _B_ref(xn, B_w_gate, B_w_up, B_w_down)
```

```cpp
#include <hip/hip_runtime.h>
#include <hip/hip_bf16.h>
#include <stdint.h>

typedef unsigned short u16;
typedef __attribute__((ext_vector_type(4))) unsigned int u32x4;
typedef __attribute__((ext_vector_type(4))) float f32x4;

constexpr int DM = 448;
constexpr int HQ = 16, HKV = 8, DH = 256, SK = 65536, WIN = 4096;
constexpr int FF = 1792;
constexpr int NCHUNK = 32, CHUNK = WIN / NCHUNK;

constexpr size_t WS_Q     = 0;
constexpr size_t WS_H     = WS_Q + 4096 * 4;
constexpr size_t WS_PML   = WS_H + 2048 * 4;
constexpr size_t WS_PO    = WS_PML + 8 * 32 * 4 * 4;
constexpr size_t WS_FPART = WS_PO + (size_t)8 * 32 * 2 * 256 * 4;
constexpr size_t WS_APART = WS_FPART + 16 * 448 * 4;
constexpr size_t WS_CNT   = WS_APART + 32 * 448 * 4;

__device__ __forceinline__ float bf2f(u16 v) { return __uint_as_float(((unsigned)v) << 16); }
__device__ __forceinline__ u16 f2bf(float f) {
  unsigned u = __float_as_uint(f);
  if ((u & 0x7fffffffu) > 0x7f800000u) return (u16)((u >> 16) | 0x40);
  u += 0x7fffu + ((u >> 16) & 1u);
  return (u16)(u >> 16);
}
__device__ __forceinline__ float rbf(float f) { return bf2f(f2bf(f)); }

__device__ __forceinline__ float wave_sum(float v) {
#pragma unroll
  for (int o = 32; o > 0; o >>= 1) v += __shfl_xor(v, o, 64);
  return v;
}
__device__ __forceinline__ float wave_max(float v) {
#pragma unroll
  for (int o = 32; o > 0; o >>= 1) v = fmaxf(v, __shfl_xor(v, o, 64));
  return v;
}

__device__ __forceinline__ void layernorm_to_lds(const u16* __restrict__ x, float* xn, float* red, int t) {
  float a0 = (t < DM) ? bf2f(x[t]) : 0.f;
  float a1 = (t + 256 < DM) ? bf2f(x[t + 256]) : 0.f;
  float s = wave_sum(a0 + a1);
  if ((t & 63) == 0) red[t >> 6] = s;
  __syncthreads();
  float mean = (red[0] + red[1] + red[2] + red[3]) * (1.0f / DM);
  float d0 = (t < DM) ? a0 - mean : 0.f;
  float d1 = (t + 256 < DM) ? a1 - mean : 0.f;
  float v = wave_sum(d0 * d0 + d1 * d1);
  __syncthreads();
  if ((t & 63) == 0) red[4 + (t >> 6)] = v;
  __syncthreads();
  float var = (red[4] + red[5] + red[6] + red[7]) * (1.0f / DM);
  float mb = rbf(mean), vb = rbf(var);
  float den = rbf(sqrtf(rbf(vb + 1e-5f)));
  if (t < DM) xn[t] = rbf(rbf(a0 - mb) / den);
  if (t + 256 < DM) xn[t + 256] = rbf(rbf(a1 - mb) / den);
  __syncthreads();
}

__device__ __forceinline__ void fma8(float (&acc)[8], u32x4 w, float s) {
#pragma unroll
  for (int k = 0; k < 4; ++k) {
    acc[2 * k]     = fmaf(s, __uint_as_float(w[k] << 16), acc[2 * k]);
    acc[2 * k + 1] = fmaf(s, __uint_as_float(w[k] & 0xffff0000u), acc[2 * k + 1]);
  }
}

__global__ void __launch_bounds__(256) k1_proj(const u16* __restrict__ x, const u16* __restrict__ wq,
                                               const u16* __restrict__ wg, const u16* __restrict__ wu,
                                               float* __restrict__ qbuf, float* __restrict__ hbuf,
                                               unsigned* __restrict__ cnt) {
  __shared__ float xn[DM];
  __shared__ float red[8];
  __shared__ float wred[2][4][32];
  const int t = threadIdx.x, b = blockIdx.x;
  const int cg = t & 3, r = t >> 2, lane = t & 63, wave = t >> 6;
  if (b == 0 && t < 8) cnt[t * 32] = 0u;
  if (b < 128) {
    const u16* wp = wq + (size_t)r * 4096 + b * 32 + cg * 8;
    u32x4 w[7];
#pragma unroll
    for (int i = 0; i < 7; ++i) w[i] = __builtin_nontemporal_load((const u32x4*)(wp + (size_t)i * 64 * 4096));
    layernorm_to_lds(x, xn, red, t);
    float acc[8] = {0, 0, 0, 0, 0, 0, 0, 0};
#pragma unroll
    for (int i = 0; i < 7; ++i) fma8(acc, w[i], xn[r + 64 * i]);
#pragma unroll
    for (int k = 0; k < 8; ++k) {
      float v = acc[k];
      v += __shfl_xor(v, 4, 64); v += __shfl_xor(v, 8, 64); v += __shfl_xor(v, 16, 64); v += __shfl_xor(v, 32, 64);
      acc[k] = v;
    }
    if (lane < 4) {
#pragma unroll
      for (int k = 0; k < 8; ++k) wred[0][wave][lane * 8 + k] = acc[k];
    }
    __syncthreads();
    if (t < 32) {
      float v = wred[0][0][t] + wred[0][1][t] + wred[0][2][t] + wred[0][3][t];
      qbuf[b * 32 + t] = rbf(v);
    }
  } else {
    const int fb = b - 128;
    const u16* gp = wg + (size_t)r * FF + fb * 32 + cg * 8;
    const u16* up = wu + (size_t)r * FF + fb * 32 + cg * 8;
    u32x4 w[7], z[7];
#pragma unroll
    for (int i = 0; i < 7; ++i) {
      w[i] = __builtin_nontemporal_load((const u32x4*)(gp + (size_t)i * 64 * FF));
      z[i] = __builtin_nontemporal_load((const u32x4*)(up + (size_t)i * 64 * FF));
    }
    layernorm_to_lds(x, xn, red, t);
    float ag[8] = {0, 0, 0, 0, 0, 0, 0, 0}, au[8] = {0, 0, 0, 0, 0, 0, 0, 0};
#pragma unroll
    for (int i = 0; i < 7; ++i) { float s = xn[r + 64 * i]; fma8(ag, w[i], s); fma8(au, z[i], s); }
#pragma unroll
    for (int k = 0; k < 8; ++k) {
      float v = ag[k], u = au[k];
      v += __shfl_xor(v, 4, 64); v += __shfl_xor(v, 8, 64); v += __shfl_xor(v, 16, 64); v += __shfl_xor(v, 32, 64);
      u += __shfl_xor(u, 4, 64); u += __shfl_xor(u, 8, 64); u += __shfl_xor(u, 16, 64); u += __shfl_xor(u, 32, 64);
      ag[k] = v; au[k] = u;
    }
    if (lane < 4) {
#pragma unroll
      for (int k = 0; k < 8; ++k) { wred[0][wave][lane * 8 + k] = ag[k]; wred[1][wave][lane * 8 + k] = au[k]; }
    }
    __syncthreads();
    if (t < 32) {
      float g = rbf(wred[0][0][t] + wred[0][1][t] + wred[0][2][t] + wred[0][3][t]);
      float u = rbf(wred[1][0][t] + wred[1][1][t] + wred[1][2][t] + wred[1][3][t]);
      float sg = rbf(1.0f / (1.0f + expf(-g)));
      hbuf[fb * 32 + t] = rbf(sg * u);
    }
  }
}

__global__ void __launch_bounds__(256) k2_attn(const u16* __restrict__ kc, const u16* __restrict__ vc,
                                               const float* __restrict__ qbuf, const float* __restrict__ hbuf,
                                               const u16* __restrict__ wd,
                                               float* __restrict__ pml, float* __restrict__ po,
                                               float* __restrict__ fpart) {
  __shared__ float sc[2][CHUNK];
  __shared__ float ored[4][2][DH];
  __shared__ float stat[2][2];
  const int t = threadIdx.x, b = blockIdx.x, lane = t & 63, wave = t >> 6;
  if (b < 256) {
    const int kvh = b >> 5, ch = b & 31;
    const size_t base = ((size_t)kvh * SK + (SK - WIN) + (size_t)ch * CHUNK) * DH;
    const int half = lane >> 5, d8 = (lane & 31) * 8;
    const u16* kp = kc + base + (size_t)(wave * 32 + half) * DH + d8;
    const u16* vp = vc + base + (size_t)(wave * 32 + half) * DH + d8;
    u32x4 K[16], V[16];
#pragma unroll
    for (int i = 0; i < 16; ++i) K[i] = __builtin_nontemporal_load((const u32x4*)(kp + (size_t)i * 2 * DH));
#pragma unroll
    for (int i = 0; i < 16; ++i) V[i] = __builtin_nontemporal_load((const u32x4*)(vp + (size_t)i * 2 * DH));
    float q0[8], q1[8];
#pragma unroll
    for (int k = 0; k < 8; ++k) { q0[k] = qbuf[(2 * kvh) * DH + d8 + k]; q1[k] = qbuf[(2 * kvh + 1) * DH + d8 + k]; }
#pragma unroll
    for (int i = 0; i < 16; ++i) {
      float s0 = 0.f, s1 = 0.f;
#pragma unroll
      for (int k = 0; k < 4; ++k) {
        float lo = __uint_as_float(K[i][k] << 16), hi = __uint_as_float(K[i][k] & 0xffff0000u);
        s0 = fmaf(lo, q0[2 * k], s0); s0 = fmaf(hi, q0[2 * k + 1], s0);
        s1 = fmaf(lo, q1[2 * k], s1); s1 = fmaf(hi, q1[2 * k + 1], s1);
      }
#pragma unroll
      for (int o = 16; o > 0; o >>= 1) { s0 += __shfl_xor(s0, o, 64); s1 += __shfl_xor(s1, o, 64); }
      if ((lane & 31) == 0) {
        int row = wave * 32 + 2 * i + half;
        sc[0][row] = rbf(s0) * 0.0625f;
        sc[1][row] = rbf(s1) * 0.0625f;
      }
    }
    __syncthreads();
    if (wave < 2) {
      float a = sc[wave][lane], c = sc[wave][lane + 64];
      float m = wave_max(fmaxf(a, c));
      float ea = expf(a - m), ec = expf(c - m);
      float l = wave_sum(ea + ec);
      sc[wave][lane] = ea; sc[wave][lane + 64] = ec;
      if (lane == 0) { stat[wave][0] = m; stat[wave][1] = l; }
    }
    __syncthreads();
    float a0[8] = {0, 0, 0, 0, 0, 0, 0, 0}, a1[8] = {0, 0, 0, 0, 0, 0, 0, 0};
#pragma unroll
    for (int i = 0; i < 16; ++i) {
      int row = wave * 32 + 2 * i + half;
      fma8(a0, V[i], sc[0][row]);
      fma8(a1, V[i], sc[1][row]);
    }
#pragma unroll
    for (int k = 0; k < 8; ++k) { a0[k] += __shfl_xor(a0[k], 32, 64); a1[k] += __shfl_xor(a1[k], 32, 64); }
    if (lane < 32) {
#pragma unroll
      for (int k = 0; k < 8; ++k) { ored[wave][0][d8 + k] = a0[k]; ored[wave][1][d8 + k] = a1[k]; }
    }
    __syncthreads();
    float* pod = po + (size_t)((kvh * NCHUNK + ch) * 2) * DH;
    for (int j = t; j < 2 * DH; j += 256) {
      int h = j >> 8, d = j & 255;
      pod[j] = ored[0][h][d] + ored[1][h][d] + ored[2][h][d] + ored[3][h][d];
    }
    if (t < 4) pml[(kvh * NCHUNK + ch) * 4 + t] = stat[t >> 1][t & 1];
  } else {
    const int fb = b - 256, cg = fb % 7, rg = fb / 7;
    const int c8 = t & 7, r = t >> 3;
    const u16* wp = wd + (size_t)(rg * 128 + r) * DM + cg * 64 + c8 * 8;
    u32x4 w[4];
#pragma unroll
    for (int i = 0; i < 4; ++i) w[i] = __builtin_nontemporal_load((const u32x4*)(wp + (size_t)i * 32 * DM));
    float acc[8] = {0, 0, 0, 0, 0, 0, 0, 0};
#pragma unroll
    for (int i = 0; i < 4; ++i) fma8(acc, w[i], hbuf[rg * 128 + r + 32 * i]);
#pragma unroll
    for (int k = 0; k < 8; ++k) {
      float v = acc[k];
      v += __shfl_xor(v, 8, 64); v += __shfl_xor(v, 16, 64); v += __shfl_xor(v, 32, 64);
      acc[k] = v;
    }
    float* fr = &ored[0][0][0];
    if (lane < 8) {
#pragma unroll
      for (int k = 0; k < 8; ++k) fr[wave * 64 + lane * 8 + k] = acc[k];
    }
    __syncthreads();
    if (t < 64) fpart[rg * DM + cg * 64 + t] = fr[t] + fr[64 + t] + fr[128 + t] + fr[192 + t];
  }
}

__global__ void __launch_bounds__(256) k3_out(const u16* __restrict__ x, const u16* __restrict__ wo,
                                              const float* __restrict__ pml, const float* __restrict__ po,
                                              const float* __restrict__ fpart, float* __restrict__ apart,
                                              unsigned* __restrict__ cnt, u16* __restrict__ out) {
  __shared__ float o_s[128];
  __shared__ float wsc[NCHUNK];
  __shared__ float fr[4 * 64];
  __shared__ unsigned last_s;
  const int t = threadIdx.x, b = blockIdx.x, lane = t & 63, wave = t >> 6;
  const int cg = b % 7, rg = b / 7;
  const int h = rg >> 1, kvh = h >> 1, hh = h & 1, d0 = (rg & 1) * 128;
  const int c8 = t & 7, r = t >> 3;
  const u16* wp = wo + (size_t)(rg * 128 + r) * DM + cg * 64 + c8 * 8;
  u32x4 w[4];
#pragma unroll
  for (int i = 0; i < 4; ++i) w[i] = __builtin_nontemporal_load((const u32x4*)(wp + (size_t)i * 32 * DM));
  if (wave == 0) {
    float m = (lane < NCHUNK) ? pml[(kvh * NCHUNK + lane) * 4 + hh * 2] : -INFINITY;
    float l = (lane < NCHUNK) ? pml[(kvh * NCHUNK + lane) * 4 + hh * 2 + 1] : 0.f;
    float M = wave_max(m);
    float e = (lane < NCHUNK) ? expf(m - M) : 0.f;
    float L = wave_sum(e * l);
    if (lane < NCHUNK) wsc[lane] = e / L;
  }
  __syncthreads();
  {
    const int d = t & 127, chh = t >> 7;
    float s = 0.f;
    for (int c = chh * 16; c < chh * 16 + 16; ++c)
      s = fmaf(wsc[c], po[(size_t)((kvh * NCHUNK + c) * 2 + hh) * DH + d0 + d], s);
    fr[t] = s;
  }
  __syncthreads();
  if (t < 128) o_s[t] = rbf(fr[t] + fr[t + 128]);
  __syncthreads();
  float acc[8] = {0, 0, 0, 0, 0, 0, 0, 0};
#pragma unroll
  for (int i = 0; i < 4; ++i) fma8(acc, w[i], o_s[r + 32 * i]);
#pragma unroll
  for (int k = 0; k < 8; ++k) {
    float v = acc[k];
    v += __shfl_xor(v, 8, 64); v += __shfl_xor(v, 16, 64); v += __shfl_xor(v, 32, 64);
    acc[k] = v;
  }
  __syncthreads();
  if (lane < 8) {
#pragma unroll
    for (int k = 0; k < 8; ++k) fr[wave * 64 + lane * 8 + k] = acc[k];
  }
  __syncthreads();
  if (t < 64) {
    float v = fr[t] + fr[64 + t] + fr[128 + t] + fr[192 + t];
    __hip_atomic_store(&apart[rg * DM + cg * 64 + t], v, __ATOMIC_RELAXED, __HIP_MEMORY_SCOPE_AGENT);
  }
  asm volatile("s_waitcnt vmcnt(0)" ::: "memory");
  __syncthreads();
  if (t == 0) {
    unsigned old = __hip_atomic_fetch_add(&cnt[cg * 32], 1u, __ATOMIC_RELAXED, __HIP_MEMORY_SCOPE_AGENT);
    unsigned last = (old == 31u) ? 1u : 0u;
    if (last) __builtin_amdgcn_fence(__ATOMIC_ACQUIRE, "agent");
    asm volatile("s_waitcnt vmcnt(0)" ::: "memory");
    last_s = last;
  }
  __syncthreads();
  if (last_s && t < 64) {
    const int c = cg * 64 + t;
    float a = 0.f;
    for (int g = 0; g < 32; ++g)
      a += __hip_atomic_load(&apart[g * DM + c], __ATOMIC_RELAXED, __HIP_MEMORY_SCOPE_AGENT);
    float f = 0.f;
    for (int g = 0; g < 14; ++g) f += fpart[g * DM + c];
    float xo = bf2f(x[c]);
    out[c] = f2bf(rbf(xo + rbf(a)) + rbf(f));
  }
}

extern "C" void kernel_launch(void* const* d_in, const int* in_sizes, int n_in,
                              void* d_out, int out_size, void* d_ws, size_t ws_size,
                              hipStream_t stream) {
  const u16* x  = (const u16*)d_in[0];
  const u16* kc = (const u16*)d_in[1];
  const u16* vc = (const u16*)d_in[2];
  const u16* wq = (const u16*)d_in[3];
  const u16* wo = (const u16*)d_in[4];
  const u16* wg = (const u16*)d_in[6];
  const u16* wu = (const u16*)d_in[7];
  const u16* wd = (const u16*)d_in[8];
  char* ws = (char*)d_ws;
  float* qbuf = (float*)(ws + WS_Q);
  float* hbuf = (float*)(ws + WS_H);
  float* pml = (float*)(ws + WS_PML);
  float* po = (float*)(ws + WS_PO);
  float* fpart = (float*)(ws + WS_FPART);
  float* apart = (float*)(ws + WS_APART);
  unsigned* cnt = (unsigned*)(ws + WS_CNT);
  k1_proj<<<128 + 56, 256, 0, stream>>>(x, wq, wg, wu, qbuf, hbuf, cnt);
  k2_attn<<<256 + 98, 256, 0, stream>>>(kc, vc, qbuf, hbuf, wd, pml, po, fpart);
  k3_out<<<224, 256, 0, stream>>>(x, wo, pml, po, fpart, apart, cnt, (u16*)d_out);
}
```

```cpp
#include <hip/hip_runtime.h>
#include <hip/hip_bf16.h>
#include <stdint.h>

typedef unsigned short u16;
typedef unsigned long long u64;
typedef __attribute__((ext_vector_type(4))) unsigned int u32x4;

constexpr int DM = 448, DH = 256, SK = 65536, WIN = 4096, FF = 1792;
constexpr int NCHUNK = 32, CHUNK = 128;
constexpr int NWG = 256, NTH = 576;
constexpr unsigned SPIN_CAP = 1u << 18;

constexpr size_t G_Q   = 0;
constexpr size_t G_ML  = G_Q + 8 * 256;
constexpr size_t G_PO  = G_ML + 256 * 4;
constexpr size_t G_FR  = G_PO + 256 * 512;
constexpr size_t G_AP  = G_FR + 224 * 448;
constexpr size_t G_FP  = G_AP + 32 * 448;
constexpr size_t G_END = G_FP + 32 * 448;

__device__ __forceinline__ float bf2f(u16 v) { return __uint_as_float(((unsigned)v) << 16); }
__device__ __forceinline__ u16 f2bf(float f) {
  unsigned u = __float_as_uint(f);
  if ((u & 0x7fffffffu) > 0x7f800000u) return (u16)((u >> 16) | 0x40);
  u += 0x7fffu + ((u >> 16) & 1u);
  return (u16)(u >> 16);
}
__device__ __forceinline__ float rbf(float f) { return bf2f(f2bf(f)); }

template <int CTRL> __device__ __forceinline__ float dpp(float x) {
  return __builtin_bit_cast(float, __builtin_amdgcn_update_dpp(0, __builtin_bit_cast(int, x), CTRL, 0xf, 0xf, true));
}
__device__ __forceinline__ float xsum16_32(float x) {
  auto s = __builtin_amdgcn_permlane16_swap(__float_as_uint(x), __float_as_uint(x), false, false);
  x = __uint_as_float(s[0]) + __uint_as_float(s[1]);
  auto t = __builtin_amdgcn_permlane32_swap(__float_as_uint(x), __float_as_uint(x), false, false);
  return __uint_as_float(t[0]) + __uint_as_float(t[1]);
}
__device__ __forceinline__ float xmax16_32(float x) {
  auto s = __builtin_amdgcn_permlane16_swap(__float_as_uint(x), __float_as_uint(x), false, false);
  x = fmaxf(__uint_as_float(s[0]), __uint_as_float(s[1]));
  auto t = __builtin_amdgcn_permlane32_swap(__float_as_uint(x), __float_as_uint(x), false, false);
  return fmaxf(__uint_as_float(t[0]), __uint_as_float(t[1]));
}
__device__ __forceinline__ float wave_sum(float v) {
  v += dpp<0xB1>(v); v += dpp<0x4E>(v); v += dpp<0x141>(v); v += dpp<0x140>(v);
  return xsum16_32(v);
}
__device__ __forceinline__ float wave_max(float v) {
  v = fmaxf(v, dpp<0xB1>(v)); v = fmaxf(v, dpp<0x4E>(v)); v = fmaxf(v, dpp<0x141>(v)); v = fmaxf(v, dpp<0x140>(v));
  return xmax16_32(v);
}

__device__ __forceinline__ void gstore(u64* p, float v) {
  __hip_atomic_store(p, (1ull << 32) | (u64)__float_as_uint(v), __ATOMIC_RELAXED, __HIP_MEMORY_SCOPE_AGENT);
}
__device__ __forceinline__ void gstore_u(u64* p, unsigned v) {
  __hip_atomic_store(p, (1ull << 32) | (u64)v, __ATOMIC_RELAXED, __HIP_MEMORY_SCOPE_AGENT);
}
__device__ __forceinline__ u64 gload(const u64* p) {
  return __hip_atomic_load(p, __ATOMIC_RELAXED, __HIP_MEMORY_SCOPE_AGENT);
}
__device__ __forceinline__ void bar() {
  asm volatile("s_waitcnt lgkmcnt(0)" ::: "memory");
  __builtin_amdgcn_s_barrier();
  asm volatile("" ::: "memory");
}
__device__ __forceinline__ void fma8(float (&acc)[8], u32x4 w, float s) {
#pragma unroll
  for (int k = 0; k < 4; ++k) {
    acc[2 * k]     = fmaf(s, __uint_as_float(w[k] << 16), acc[2 * k]);
    acc[2 * k + 1] = fmaf(s, __uint_as_float(w[k] & 0xffff0000u), acc[2 * k + 1]);
  }
}
__device__ __forceinline__ u32x4 ldnt(const u16* p) { return __builtin_nontemporal_load((const u32x4*)p); }

struct LdsT {
  float xn[DM];
  float qred[8][16];
  float gred[8][16];
  float qs[512];
  float sc[2][CHUNK];
  float ored[8][2][DH];
};

__global__ void __launch_bounds__(NTH, 1)
fused_kernel(const u16* __restrict__ x, const u16* __restrict__ kc, const u16* __restrict__ vc,
             const u16* __restrict__ wq, const u16* __restrict__ wo, const u16* __restrict__ wg,
             const u16* __restrict__ wu, const u16* __restrict__ wd, u64* __restrict__ G,
             u16* __restrict__ out) {
  __shared__ LdsT L;
  const int t = threadIdx.x, b = blockIdx.x, lane = t & 63, wave = t >> 6;
  const int kvh = b >> 5, ch = b & 31;
  const bool hasF = b < 224;
  const int cg = b % 7, rg = b / 7;

  if (wave < 8) {
    u32x4 xv = *(const u32x4*)(x + min(lane, 55) * 8);
    __builtin_amdgcn_sched_barrier(0);
    const int qrow = t >> 1, qcol = kvh * 512 + ch * 16 + (t & 1) * 8;
    u32x4 wq0 = ldnt(wq + (size_t)qrow * 4096 + qcol);
    u32x4 wq1 = ldnt(wq + (size_t)min(qrow + 256, DM - 1) * 4096 + qcol);
    const int frow = min(t, DM - 1), fcol = hasF ? b * 8 : 0;
    u32x4 wgv = ldnt(wg + (size_t)frow * FF + fcol), wuv = ldnt(wu + (size_t)frow * FF + fcol);
    const int half = lane >> 5, d8 = (lane & 31) * 8;
    const size_t base = ((size_t)kvh * SK + (SK - WIN) + (size_t)ch * CHUNK + wave * 16 + half) * DH + d8;
    u32x4 K[8], V[8];
#pragma unroll
    for (int i = 0; i < 8; ++i) K[i] = ldnt(kc + base + (size_t)i * 2 * DH);
#pragma unroll
    for (int i = 0; i < 8; ++i) V[i] = ldnt(vc + base + (size_t)i * 2 * DH);
    u32x4 wov[2] = {{0, 0, 0, 0}, {0, 0, 0, 0}};
    if (hasF) {
      const u16* wp = wo + (size_t)(rg * 128 + (t >> 3)) * DM + cg * 64 + (t & 7) * 8;
      wov[0] = ldnt(wp); wov[1] = ldnt(wp + (size_t)64 * DM);
    }
    {
      float xf[8];
      __builtin_amdgcn_sched_barrier(0);
      if (lane >= 56) xv = u32x4{0, 0, 0, 0};
#pragma unroll
      for (int k = 0; k < 4; ++k) { xf[2 * k] = __uint_as_float(xv[k] << 16); xf[2 * k + 1] = __uint_as_float(xv[k] & 0xffff0000u); }
      float s = 0.f;
#pragma unroll
      for (int k = 0; k < 8; ++k) s += xf[k];
      float mean = wave_sum(s) * (1.0f / DM);
      float v = 0.f;
      if (lane < 56) {
#pragma unroll
        for (int k = 0; k < 8; ++k) { float d = xf[k] - mean; v = fmaf(d, d, v); }
      }
      float var = wave_sum(v) * (1.0f / DM);
      float mb = rbf(mean), vb = rbf(var);
      float den = rbf(sqrtf(rbf(vb + 1e-5f)));
      if (lane < 56) {
#pragma unroll
        for (int k = 0; k < 8; ++k) L.xn[lane * 8 + k] = rbf(rbf(xf[k] - mb) / den);
      }
      asm volatile("s_waitcnt lgkmcnt(0)" ::: "memory");
    }
    {
      float acc[8] = {0, 0, 0, 0, 0, 0, 0, 0};
      fma8(acc, wq0, L.xn[qrow]);
      fma8(acc, wq1, (t < 384) ? L.xn[min(qrow + 256, DM - 1)] : 0.f);
#pragma unroll
      for (int k = 0; k < 8; ++k) {
        float v = acc[k];
        v += __shfl_xor(v, 2, 64); v += __shfl_xor(v, 4, 64); v += __shfl_xor(v, 8, 64);
        v += __shfl_xor(v, 16, 64); v += __shfl_xor(v, 32, 64);
        acc[k] = v;
      }
      if (lane < 2) {
#pragma unroll
        for (int k = 0; k < 8; ++k) L.qred[wave][lane * 8 + k] = acc[k];
      }
      if (hasF) {
        float ag[8] = {0, 0, 0, 0, 0, 0, 0, 0}, au[8] = {0, 0, 0, 0, 0, 0, 0, 0};
        { float s = (t < DM) ? L.xn[frow] : 0.f; fma8(ag, wgv, s); fma8(au, wuv, s); }
#pragma unroll
        for (int k = 0; k < 8; ++k) { ag[k] = wave_sum(ag[k]); au[k] = wave_sum(au[k]); }
        if (lane == 0) {
#pragma unroll
          for (int k = 0; k < 8; ++k) { L.gred[wave][k] = ag[k]; L.gred[wave][8 + k] = au[k]; }
        }
      }
    }
    bar();
    bar();
    float q0[8], q1[8];
#pragma unroll
    for (int k = 0; k < 8; ++k) { q0[k] = L.qs[d8 + k]; q1[k] = L.qs[256 + d8 + k]; }
    float sv[16];
#pragma unroll
    for (int i = 0; i < 8; ++i) {
      float s0 = 0.f, s1 = 0.f;
#pragma unroll
      for (int k = 0; k < 4; ++k) {
        float lo = __uint_as_float(K[i][k] << 16), hi = __uint_as_float(K[i][k] & 0xffff0000u);
        s0 = fmaf(lo, q0[2 * k], s0); s0 = fmaf(hi, q0[2 * k + 1], s0);
        s1 = fmaf(lo, q1[2 * k], s1); s1 = fmaf(hi, q1[2 * k + 1], s1);
      }
      sv[2 * i] = s0; sv[2 * i + 1] = s1;
    }
#pragma unroll
    for (int st = 0; st < 4; ++st) {
      const int n = 8 >> st, bit = 16 >> st;
#pragma unroll
      for (int j = 0; j < n; ++j) {
        float lo = sv[j], hi = sv[j + n];
        float keep = (lane & bit) ? hi : lo, give = (lane & bit) ? lo : hi;
        sv[j] = keep + __shfl_xor(give, bit, 64);
      }
    }
    {
      float s = sv[0] + __shfl_xor(sv[0], 1, 64);
      int vi = ((lane >> 4) & 1) * 8 + ((lane >> 3) & 1) * 4 + ((lane >> 2) & 1) * 2 + ((lane >> 1) & 1);
      int row = wave * 16 + (vi >> 1) * 2 + half, hh = vi & 1;
      if ((lane & 1) == 0) L.sc[hh][row] = rbf(s) * 0.0625f;
    }
    bar();
    float m0, m1, l0, l1;
    {
      float a0 = L.sc[0][lane], a1 = L.sc[0][lane + 64], c0 = L.sc[1][lane], c1 = L.sc[1][lane + 64];
      m0 = wave_max(fmaxf(a0, a1)); m1 = wave_max(fmaxf(c0, c1));
      l0 = wave_sum(__expf(a0 - m0) + __expf(a1 - m0));
      l1 = wave_sum(__expf(c0 - m1) + __expf(c1 - m1));
    }
    float a0[8] = {0, 0, 0, 0, 0, 0, 0, 0}, a1[8] = {0, 0, 0, 0, 0, 0, 0, 0};
#pragma unroll
    for (int i = 0; i < 8; ++i) {
      int row = wave * 16 + 2 * i + half;
      float p0 = __expf(L.sc[0][row] - m0), p1 = __expf(L.sc[1][row] - m1);
      fma8(a0, V[i], p0);
      fma8(a1, V[i], p1);
    }
#pragma unroll
    for (int k = 0; k < 8; ++k) { a0[k] += __shfl_xor(a0[k], 32, 64); a1[k] += __shfl_xor(a1[k], 32, 64); }
    if (lane < 32) {
#pragma unroll
      for (int k = 0; k < 8; ++k) { L.ored[wave][0][d8 + k] = a0[k]; L.ored[wave][1][d8 + k] = a1[k]; }
    }
    bar();
    {
      const float* o = &L.ored[0][0][0];
      float s = 0.f;
#pragma unroll
      for (int w = 0; w < 8; ++w) s += o[w * 512 + t];
      gstore(G + G_PO + (size_t)b * 512 + t, s);
      if (t < 4) gstore(G + G_ML + b * 4 + t, t == 0 ? m0 : t == 1 ? l0 : t == 2 ? m1 : l1);
    }
    bar();
    if (!hasF) return;
    const int h = rg >> 1, kv3 = h >> 1, hh = h & 1, d0 = (rg & 1) * 128;
    float wgt8[8];
    {
      const int d = t & 127, cq = t >> 7;
      float ov[8], mm = 0.f, ll = 0.f;
      unsigned spins = 0;
      for (;;) {
        bool ok = true;
        u64 gm = (1ull << 32), gl = (1ull << 32);
        if (lane < 32) { gm = gload(G + G_ML + (kv3 * 32 + lane) * 4 + hh * 2); gl = gload(G + G_ML + (kv3 * 32 + lane) * 4 + hh * 2 + 1); }
#pragma unroll
        for (int j = 0; j < 8; ++j) {
          u64 g = gload(G + G_PO + (size_t)(kv3 * 32 + cq * 8 + j) * 512 + hh * 256 + d0 + d);
          ov[j] = __uint_as_float((unsigned)g); ok &= (g >> 32) == 1ull;
        }
        ok &= ((gm >> 32) == 1ull) && ((gl >> 32) == 1ull);
        mm = __uint_as_float((unsigned)gm); ll = __uint_as_float((unsigned)gl);
        if (__all(ok)) break;
        if (++spins > SPIN_CAP) break;
        __builtin_amdgcn_s_sleep(1);
      }
      if (lane >= 32) { mm = -INFINITY; ll = 0.f; }
      float M = wave_max(mm);
      float e = (lane < 32) ? __expf(mm - M) : 0.f;
      float Lsum = wave_sum(e * ll);
      float wv = e / Lsum;
      float s = 0.f;
#pragma unroll
      for (int j = 0; j < 8; ++j) s = fmaf(__shfl(wv, cq * 8 + j, 64), ov[j], s);
      float* fr = &L.ored[0][0][0];
      fr[t] = s;
      (void)wgt8;
    }
    bar();
    {
      const float* fr = &L.ored[0][0][0];
      const int r = t >> 3;
      float o0 = rbf(fr[r] + fr[128 + r] + fr[256 + r] + fr[384 + r]);
      float o1 = rbf(fr[64 + r] + fr[192 + r] + fr[320 + r] + fr[448 + r]);
      float acc[8] = {0, 0, 0, 0, 0, 0, 0, 0};
      fma8(acc, wov[0], o0); fma8(acc, wov[1], o1);
#pragma unroll
      for (int k = 0; k < 8; ++k) {
        float v = acc[k];
        v += __shfl_xor(v, 8, 64); v += __shfl_xor(v, 16, 64); v += __shfl_xor(v, 32, 64);
        acc[k] = v;
      }
      float* red = &L.ored[2][0][0];
      if (lane < 8) {
#pragma unroll
        for (int k = 0; k < 8; ++k) red[wave * 64 + lane * 8 + k] = acc[k];
      }
    }
    bar();
    if (wave == 0) {
      const float* red = &L.ored[2][0][0];
      float a = 0.f;
#pragma unroll
      for (int w = 0; w < 8; ++w) a += red[w * 64 + lane];
      float f = 0.f;
      unsigned spins = 0;
      for (;;) {
        bool ok = true; f = 0.f;
#pragma unroll
        for (int j = 0; j < 7; ++j) {
          u64 g = gload(G + G_FR + (size_t)(rg * 7 + j) * DM + cg * 64 + lane);
          f += __uint_as_float((unsigned)g); ok &= (g >> 32) == 1ull;
        }
        if (__all(ok)) break;
        if (++spins > SPIN_CAP) break;
        __builtin_amdgcn_s_sleep(1);
      }
      gstore(G + G_AP + rg * DM + cg * 64 + lane, a);
      gstore(G + G_FP + rg * DM + cg * 64 + lane, f);
    }
    if (rg != 0) return;
    {
      float a = 0.f, f = 0.f;
      unsigned spins = 0;
      for (;;) {
        bool ok = true; a = 0.f; f = 0.f;
#pragma unroll
        for (int j = 0; j < 4; ++j) {
          u64 ga = gload(G + G_AP + (wave * 4 + j) * DM + cg * 64 + lane);
          u64 gf = gload(G + G_FP + (wave * 4 + j) * DM + cg * 64 + lane);
          a += __uint_as_float((unsigned)ga); f += __uint_as_float((unsigned)gf);
          ok &= ((ga >> 32) == 1ull) && ((gf >> 32) == 1ull);
        }
        if (__all(ok)) break;
        if (++spins > SPIN_CAP) break;
        __builtin_amdgcn_s_sleep(1);
      }
      float* red = &L.ored[4][0][0];
      red[wave * 128 + lane] = a; red[wave * 128 + 64 + lane] = f;
    }
    bar();
    if (wave == 0) {
      const float* red = &L.ored[4][0][0];
      float a = 0.f, f = 0.f;
#pragma unroll
      for (int w = 0; w < 8; ++w) { a += red[w * 128 + lane]; f += red[w * 128 + 64 + lane]; }
      const int c = cg * 64 + lane;
      float xo = bf2f(x[c]);
      out[c] = f2bf(rbf(xo + rbf(a)) + rbf(f));
    }
  } else {
    u32x4 wdv[8];
#pragma unroll
    for (int i = 0; i < 8; ++i) wdv[i] = u32x4{0, 0, 0, 0};
    if (hasF && lane < 56) {
#pragma unroll
      for (int i = 0; i < 8; ++i) wdv[i] = ldnt(wd + (size_t)(b * 8 + i) * DM + lane * 8);
    }
    bar();
    {
      float v = 0.f;
      if (lane < 16) {
#pragma unroll
        for (int w = 0; w < 8; ++w) v += L.qred[w][lane];
      }
      unsigned qb = f2bf(v);
      unsigned hi = __shfl_down(qb, 1, 64);
      if (lane < 16 && (lane & 1) == 0) gstore_u(G + G_Q + kvh * 256 + ch * 8 + (lane >> 1), qb | (hi << 16));
    }
    if (hasF) {
      float g = 0.f, u = 0.f;
      if (lane < 8) {
#pragma unroll
        for (int w = 0; w < 8; ++w) { g += L.gred[w][lane]; u += L.gred[w][8 + lane]; }
      }
      g = rbf(g); u = rbf(u);
      float hv = rbf(rbf(1.0f / (1.0f + expf(-g))) * u);
      float acc[8] = {0, 0, 0, 0, 0, 0, 0, 0};
#pragma unroll
      for (int i = 0; i < 8; ++i) fma8(acc, wdv[i], __shfl(hv, i, 64));
      if (lane < 56) {
#pragma unroll
        for (int k = 0; k < 8; ++k) gstore(G + G_FR + (size_t)b * DM + lane * 8 + k, acc[k]);
      }
    }
    {
      unsigned spins = 0;
      unsigned qv[4];
      for (;;) {
        bool ok = true;
#pragma unroll
        for (int j = 0; j < 4; ++j) {
          u64 g = gload(G + G_Q + kvh * 256 + j * 64 + lane);
          qv[j] = (unsigned)g; ok &= (g >> 32) == 1ull;
        }
        if (__all(ok)) break;
        if (++spins > SPIN_CAP) break;
        __builtin_amdgcn_s_sleep(1);
      }
#pragma unroll
      for (int j = 0; j < 4; ++j) {
        L.qs[(j * 64 + lane) * 2] = __uint_as_float(qv[j] << 16);
        L.qs[(j * 64 + lane) * 2 + 1] = __uint_as_float(qv[j] & 0xffff0000u);
      }
    }
    bar();
    bar();
    bar();
    bar();
    if (!hasF) return;
    bar();
    bar();
    if (rg != 0) return;
    bar();
  }
}

extern "C" void kernel_launch(void* const* d_in, const int* in_sizes, int n_in,
                              void* d_out, int out_size, void* d_ws, size_t ws_size,
                              hipStream_t stream) {
  hipMemsetAsync(d_ws, 0, G_END * sizeof(u64), stream);
  fused_kernel<<<NWG, NTH, 0, stream>>>((const u16*)d_in[0], (const u16*)d_in[1], (const u16*)d_in[2],
                                        (const u16*)d_in[3], (const u16*)d_in[4], (const u16*)d_in[6],
                                        (const u16*)d_in[7], (const u16*)d_in[8], (u64*)d_ws, (u16*)d_out);
}
```

```cpp
#include <hip/hip_runtime.h>
#include <hip/hip_bf16.h>
#include <stdint.h>

typedef unsigned short u16;
typedef unsigned long long u64;
typedef __attribute__((ext_vector_type(4))) unsigned int u32x4;

constexpr int DM = 448, DH = 256, SK = 65536, WIN = 4096, FF = 1792;
constexpr int NCHUNK = 32, CHUNK = 128;
constexpr int NWG = 256, NTH = 576;
constexpr unsigned SPIN_CAP = 1u << 18;

constexpr size_t G_Q   = 0;
constexpr size_t G_GU  = G_Q + 64 * 4 * 64;
constexpr size_t G_ML  = G_GU + 28 * 8 * 128;
constexpr size_t G_PO  = G_ML + 256 * 4;
constexpr size_t G_FR  = G_PO + 256 * 512;
constexpr size_t G_AP  = G_FR + 224 * 448;
constexpr size_t G_FP  = G_AP + 32 * 448;
constexpr size_t G_END = G_FP + 32 * 448;

__device__ __forceinline__ float bf2f(u16 v) { return __uint_as_float(((unsigned)v) << 16); }
__device__ __forceinline__ u16 f2bf(float f) {
  unsigned u = __float_as_uint(f);
  if ((u & 0x7fffffffu) > 0x7f800000u) return (u16)((u >> 16) | 0x40);
  u += 0x7fffu + ((u >> 16) & 1u);
  return (u16)(u >> 16);
}
__device__ __forceinline__ float rbf(float f) { return bf2f(f2bf(f)); }

template <int CTRL> __device__ __forceinline__ float dpp(float x) {
  return __builtin_bit_cast(float, __builtin_amdgcn_update_dpp(0, __builtin_bit_cast(int, x), CTRL, 0xf, 0xf, true));
}
__device__ __forceinline__ float xsum16_32(float x) {
  auto s = __builtin_amdgcn_permlane16_swap(__float_as_uint(x), __float_as_uint(x), false, false);
  x = __uint_as_float(s[0]) + __uint_as_float(s[1]);
  auto t = __builtin_amdgcn_permlane32_swap(__float_as_uint(x), __float_as_uint(x), false, false);
  return __uint_as_float(t[0]) + __uint_as_float(t[1]);
}
__device__ __forceinline__ float xmax16_32(float x) {
  auto s = __builtin_amdgcn_permlane16_swap(__float_as_uint(x), __float_as_uint(x), false, false);
  x = fmaxf(__uint_as_float(s[0]), __uint_as_float(s[1]));
  auto t = __builtin_amdgcn_permlane32_swap(__float_as_uint(x), __float_as_uint(x), false, false);
  return fmaxf(__uint_as_float(t[0]), __uint_as_float(t[1]));
}
__device__ __forceinline__ float wave_sum(float v) {
  v += dpp<0xB1>(v); v += dpp<0x4E>(v); v += dpp<0x141>(v); v += dpp<0x140>(v);
  return xsum16_32(v);
}
__device__ __forceinline__ float wave_max(float v) {
  v = fmaxf(v, dpp<0xB1>(v)); v = fmaxf(v, dpp<0x4E>(v)); v = fmaxf(v, dpp<0x141>(v)); v = fmaxf(v, dpp<0x140>(v));
  return xmax16_32(v);
}

__device__ __forceinline__ void gstore(u64* p, float v) {
  __hip_atomic_store(p, (1ull << 32) | (u64)__float_as_uint(v), __ATOMIC_RELAXED, __HIP_MEMORY_SCOPE_AGENT);
}
__device__ __forceinline__ void gstore_u(u64* p, unsigned v) {
  __hip_atomic_store(p, (1ull << 32) | (u64)v, __ATOMIC_RELAXED, __HIP_MEMORY_SCOPE_AGENT);
}
__device__ __forceinline__ u64 gload(const u64* p) {
  return __hip_atomic_load(p, __ATOMIC_RELAXED, __HIP_MEMORY_SCOPE_AGENT);
}
__device__ __forceinline__ void bar() {
  asm volatile("s_waitcnt lgkmcnt(0)" ::: "memory");
  __builtin_amdgcn_s_barrier();
  asm volatile("" ::: "memory");
}
__device__ __forceinline__ void fma8(float (&acc)[8], u32x4 w, float s) {
#pragma unroll
  for (int k = 0; k < 4; ++k) {
    acc[2 * k]     = fmaf(s, __uint_as_float(w[k] << 16), acc[2 * k]);
    acc[2 * k + 1] = fmaf(s, __uint_as_float(w[k] & 0xffff0000u), acc[2 * k + 1]);
  }
}
__device__ __forceinline__ u32x4 ldnt(const u16* p) { return __builtin_nontemporal_load((const u32x4*)p); }

struct LdsT {
  float xn[DM];
  float qred[8][64];
  float gred[8][128];
  float qs[512];
  float sc[2][CHUNK];
  float ored[8][2][DH];
};

__global__ void __launch_bounds__(NTH, 1)
fused_kernel(const u16* __restrict__ x, const u16* __restrict__ kc, const u16* __restrict__ vc,
             const u16* __restrict__ wq, const u16* __restrict__ wo, const u16* __restrict__ wg,
             const u16* __restrict__ wu, const u16* __restrict__ wd, u64* __restrict__ G,
             u16* __restrict__ out) {
  __shared__ LdsT L;
  const int t = threadIdx.x, b = blockIdx.x, lane = t & 63, wave = t >> 6;
  const int kvh = b >> 5, ch = b & 31;
  const bool hasF = b < 224;
  const int cg = b % 7, rg = b / 7;

  if (wave < 8) {
    u32x4 xv = *(const u32x4*)(x + min(lane, 55) * 8);
    __builtin_amdgcn_sched_barrier(0);
    const int r8 = t >> 3;
    const int qrow = (b & 3) * 112 + r8, qcol = (b >> 2) * 64 + (t & 7) * 8;
    const int qrow1 = (r8 < 48) ? qrow + 64 : qrow;
    u32x4 wq0 = ldnt(wq + (size_t)qrow * 4096 + qcol);
    u32x4 wq1 = ldnt(wq + (size_t)qrow1 * 4096 + qcol);
    const int frow = (b & 7) * 56 + min(r8, 55), fcol = (hasF ? (b >> 3) * 64 : 0) + (t & 7) * 8;
    u32x4 wgv = ldnt(wg + (size_t)frow * FF + fcol), wuv = ldnt(wu + (size_t)frow * FF + fcol);
    const int half = lane >> 5, d8 = (lane & 31) * 8;
    const size_t base = ((size_t)kvh * SK + (SK - WIN) + (size_t)ch * CHUNK + wave * 16 + half) * DH + d8;
    u32x4 K[8], V[8];
    __builtin_amdgcn_sched_barrier(0);
    __builtin_amdgcn_s_barrier();
    __builtin_amdgcn_sched_barrier(0);
#pragma unroll
    for (int i = 0; i < 8; ++i) K[i] = ldnt(kc + base + (size_t)i * 2 * DH);
    {
      float xf[8];
      __builtin_amdgcn_sched_barrier(0);
      if (lane >= 56) xv = u32x4{0, 0, 0, 0};
#pragma unroll
      for (int k = 0; k < 4; ++k) { xf[2 * k] = __uint_as_float(xv[k] << 16); xf[2 * k + 1] = __uint_as_float(xv[k] & 0xffff0000u); }
      float s = 0.f;
#pragma unroll
      for (int k = 0; k < 8; ++k) s += xf[k];
      float mean = wave_sum(s) * (1.0f / DM);
      float v = 0.f;
      if (lane < 56) {
#pragma unroll
        for (int k = 0; k < 8; ++k) { float d = xf[k] - mean; v = fmaf(d, d, v); }
      }
      float var = wave_sum(v) * (1.0f / DM);
      float mb = rbf(mean), vb = rbf(var);
      float den = rbf(sqrtf(rbf(vb + 1e-5f)));
      if (lane < 56) {
#pragma unroll
        for (int k = 0; k < 8; ++k) L.xn[lane * 8 + k] = rbf(rbf(xf[k] - mb) / den);
      }
      asm volatile("s_waitcnt lgkmcnt(0)" ::: "memory");
    }
    {
      float acc[8] = {0, 0, 0, 0, 0, 0, 0, 0};
      fma8(acc, wq0, L.xn[qrow]);
      fma8(acc, wq1, (r8 < 48) ? L.xn[qrow1] : 0.f);
      float ag[8] = {0, 0, 0, 0, 0, 0, 0, 0}, au[8] = {0, 0, 0, 0, 0, 0, 0, 0};
      { float s = (r8 < 56) ? L.xn[frow] : 0.f; fma8(ag, wgv, s); fma8(au, wuv, s); }
#pragma unroll
      for (int k = 0; k < 8; ++k) {
        acc[k] = xsum16_32(acc[k] + dpp<0x128>(acc[k]));
        ag[k] = xsum16_32(ag[k] + dpp<0x128>(ag[k]));
        au[k] = xsum16_32(au[k] + dpp<0x128>(au[k]));
      }
      if (lane < 8) {
#pragma unroll
        for (int k = 0; k < 8; ++k) {
          L.qred[wave][lane * 8 + k] = acc[k];
          L.gred[wave][lane * 8 + k] = ag[k]; L.gred[wave][64 + lane * 8 + k] = au[k];
        }
      }
    }
    bar();
    __builtin_amdgcn_sched_barrier(0);
    __builtin_amdgcn_s_barrier();
    __builtin_amdgcn_sched_barrier(0);
#pragma unroll
    for (int i = 0; i < 8; ++i) V[i] = ldnt(vc + base + (size_t)i * 2 * DH);
    u32x4 wov[2] = {{0, 0, 0, 0}, {0, 0, 0, 0}};
    if (hasF) {
      const u16* wp = wo + (size_t)(rg * 128 + (t >> 3)) * DM + cg * 64 + (t & 7) * 8;
      wov[0] = ldnt(wp); wov[1] = ldnt(wp + (size_t)64 * DM);
    }
    __builtin_amdgcn_sched_barrier(0);
    bar();
    float q0[8], q1[8];
#pragma unroll
    for (int k = 0; k < 8; ++k) { q0[k] = L.qs[d8 + k]; q1[k] = L.qs[256 + d8 + k]; }
    float sv[16];
#pragma unroll
    for (int i = 0; i < 8; ++i) {
      float s0 = 0.f, s1 = 0.f;
#pragma unroll
      for (int k = 0; k < 4; ++k) {
        float lo = __uint_as_float(K[i][k] << 16), hi = __uint_as_float(K[i][k] & 0xffff0000u);
        s0 = fmaf(lo, q0[2 * k], s0); s0 = fmaf(hi, q0[2 * k + 1], s0);
        s1 = fmaf(lo, q1[2 * k], s1); s1 = fmaf(hi, q1[2 * k + 1], s1);
      }
      sv[2 * i] = s0; sv[2 * i + 1] = s1;
    }
#pragma unroll
    for (int st = 0; st < 4; ++st) {
      const int n = 8 >> st, bit = 16 >> st;
#pragma unroll
      for (int j = 0; j < n; ++j) {
        float lo = sv[j], hi = sv[j + n];
        float keep = (lane & bit) ? hi : lo, give = (lane & bit) ? lo : hi;
        sv[j] = keep + __shfl_xor(give, bit, 64);
      }
    }
    {
      float s = sv[0] + __shfl_xor(sv[0], 1, 64);
      int vi = ((lane >> 4) & 1) * 8 + ((lane >> 3) & 1) * 4 + ((lane >> 2) & 1) * 2 + ((lane >> 1) & 1);
      int row = wave * 16 + (vi >> 1) * 2 + half, hh = vi & 1;
      if ((lane & 1) == 0) L.sc[hh][row] = rbf(s) * 0.0625f;
    }
    bar();
    float m0, m1, l0, l1;
    {
      float a0 = L.sc[0][lane], a1 = L.sc[0][lane + 64], c0 = L.sc[1][lane], c1 = L.sc[1][lane + 64];
      m0 = wave_max(fmaxf(a0, a1)); m1 = wave_max(fmaxf(c0, c1));
      l0 = wave_sum(__expf(a0 - m0) + __expf(a1 - m0));
      l1 = wave_sum(__expf(c0 - m1) + __expf(c1 - m1));
    }
    float a0[8] = {0, 0, 0, 0, 0, 0, 0, 0}, a1[8] = {0, 0, 0, 0, 0, 0, 0, 0};
#pragma unroll
    for (int i = 0; i < 8; ++i) {
      int row = wave * 16 + 2 * i + half;
      float p0 = __expf(L.sc[0][row] - m0), p1 = __expf(L.sc[1][row] - m1);
      fma8(a0, V[i], p0);
      fma8(a1, V[i], p1);
    }
#pragma unroll
    for (int k = 0; k < 8; ++k) { a0[k] += __shfl_xor(a0[k], 32, 64); a1[k] += __shfl_xor(a1[k], 32, 64); }
    if (lane < 32) {
#pragma unroll
      for (int k = 0; k < 8; ++k) { L.ored[wave][0][d8 + k] = a0[k]; L.ored[wave][1][d8 + k] = a1[k]; }
    }
    bar();
    {
      const float* o = &L.ored[0][0][0];
      float s = 0.f;
#pragma unroll
      for (int w = 0; w < 8; ++w) s += o[w * 512 + t];
      gstore(G + G_PO + (size_t)b * 512 + t, s);
      if (t < 4) gstore(G + G_ML + b * 4 + t, t == 0 ? m0 : t == 1 ? l0 : t == 2 ? m1 : l1);
    }
    bar();
    if (!hasF) return;
    const int h = rg >> 1, kv3 = h >> 1, hh = h & 1, d0 = (rg & 1) * 128;
    float wgt8[8];
    {
      const int d = t & 127, cq = t >> 7;
      float ov[8], mm = 0.f, ll = 0.f;
      unsigned spins = 0;
      for (;;) {
        bool ok = true;
        u64 gm = (1ull << 32), gl = (1ull << 32);
        if (lane < 32) { gm = gload(G + G_ML + (kv3 * 32 + lane) * 4 + hh * 2); gl = gload(G + G_ML + (kv3 * 32 + lane) * 4 + hh * 2 + 1); }
#pragma unroll
        for (int j = 0; j < 8; ++j) {
          u64 g = gload(G + G_PO + (size_t)(kv3 * 32 + cq * 8 + j) * 512 + hh * 256 + d0 + d);
          ov[j] = __uint_as_float((unsigned)g); ok &= (g >> 32) == 1ull;
        }
        ok &= ((gm >> 32) == 1ull) && ((gl >> 32) == 1ull);
        mm = __uint_as_float((unsigned)gm); ll = __uint_as_float((unsigned)gl);
        if (__all(ok)) break;
        if (++spins > SPIN_CAP) break;
        __builtin_amdgcn_s_sleep(1);
      }
      if (lane >= 32) { mm = -INFINITY; ll = 0.f; }
      float M = wave_max(mm);
      float e = (lane < 32) ? __expf(mm - M) : 0.f;
      float Lsum = wave_sum(e * ll);
      float wv = e / Lsum;
      float s = 0.f;
#pragma unroll
      for (int j = 0; j < 8; ++j) s = fmaf(__shfl(wv, cq * 8 + j, 64), ov[j], s);
      float* fr = &L.ored[0][0][0];
      fr[t] = s;
      (void)wgt8;
    }
    bar();
    {
      const float* fr = &L.ored[0][0][0];
      const int r = t >> 3;
      float o0 = rbf(fr[r] + fr[128 + r] + fr[256 + r] + fr[384 + r]);
      float o1 = rbf(fr[64 + r] + fr[192 + r] + fr[320 + r] + fr[448 + r]);
      float acc[8] = {0, 0, 0, 0, 0, 0, 0, 0};
      fma8(acc, wov[0], o0); fma8(acc, wov[1], o1);
#pragma unroll
      for (int k = 0; k < 8; ++k) {
        float v = acc[k];
        v += __shfl_xor(v, 8, 64); v += __shfl_xor(v, 16, 64); v += __shfl_xor(v, 32, 64);
        acc[k] = v;
      }
      float* red = &L.ored[2][0][0];
      if (lane < 8) {
#pragma unroll
        for (int k = 0; k < 8; ++k) red[wave * 64 + lane * 8 + k] = acc[k];
      }
    }
    bar();
    if (wave == 0) {
      const float* red = &L.ored[2][0][0];
      float a = 0.f;
#pragma unroll
      for (int w = 0; w < 8; ++w) a += red[w * 64 + lane];
      float f = 0.f;
      unsigned spins = 0;
      for (;;) {
        bool ok = true; f = 0.f;
#pragma unroll
        for (int j = 0; j < 7; ++j) {
          u64 g = gload(G + G_FR + (size_t)(rg * 7 + j) * DM + cg * 64 + lane);
          f += __uint_as_float((unsigned)g); ok &= (g >> 32) == 1ull;
        }
        if (__all(ok)) break;
        if (++spins > SPIN_CAP) break;
        __builtin_amdgcn_s_sleep(1);
      }
      gstore(G + G_AP + rg * DM + cg * 64 + lane, a);
      gstore(G + G_FP + rg * DM + cg * 64 + lane, f);
    }
    if (rg != 0) return;
    {
      float a = 0.f, f = 0.f;
      unsigned spins = 0;
      for (;;) {
        bool ok = true; a = 0.f; f = 0.f;
#pragma unroll
        for (int j = 0; j < 4; ++j) {
          u64 ga = gload(G + G_AP + (wave * 4 + j) * DM + cg * 64 + lane);
          u64 gf = gload(G + G_FP + (wave * 4 + j) * DM + cg * 64 + lane);
          a += __uint_as_float((unsigned)ga); f += __uint_as_float((unsigned)gf);
          ok &= ((ga >> 32) == 1ull) && ((gf >> 32) == 1ull);
        }
        if (__all(ok)) break;
        if (++spins > SPIN_CAP) break;
        __builtin_amdgcn_s_sleep(1);
      }
      float* red = &L.ored[4][0][0];
      red[wave * 128 + lane] = a; red[wave * 128 + 64 + lane] = f;
    }
    bar();
    if (wave == 0) {
      const float* red = &L.ored[4][0][0];
      float a = 0.f, f = 0.f;
#pragma unroll
      for (int w = 0; w < 8; ++w) { a += red[w * 128 + lane]; f += red[w * 128 + 64 + lane]; }
      const int c = cg * 64 + lane;
      float xo = bf2f(x[c]);
      out[c] = f2bf(rbf(xo + rbf(a)) + rbf(f));
    }
  } else {
    u32x4 wdv[8];
#pragma unroll
    for (int i = 0; i < 8; ++i) wdv[i] = u32x4{0, 0, 0, 0};
    if (hasF && lane < 56) {
#pragma unroll
      for (int i = 0; i < 8; ++i) wdv[i] = ldnt(wd + (size_t)(b * 8 + i) * DM + lane * 8);
    }
    __builtin_amdgcn_sched_barrier(0);
    __builtin_amdgcn_s_barrier();
    __builtin_amdgcn_sched_barrier(0);
    bar();
    {
      float v = 0.f, g = 0.f, u = 0.f;
#pragma unroll
      for (int w = 0; w < 8; ++w) { v += L.qred[w][lane]; g += L.gred[w][lane]; u += L.gred[w][64 + lane]; }
      gstore(G + G_Q + (size_t)b * 64 + lane, v);
      if (hasF) { gstore(G + G_GU + (size_t)b * 128 + lane, g); gstore(G + G_GU + (size_t)b * 128 + 64 + lane, u); }
    }
    {
      unsigned spins = 0;
      float qv[8];
      for (;;) {
        bool ok = true;
        u64 gq[32];
#pragma unroll
        for (int j = 0; j < 32; ++j) gq[j] = gload(G + G_Q + (size_t)(kvh * 32 + j) * 64 + lane);
        if (spins == 0) {
          __builtin_amdgcn_sched_barrier(0);
          __builtin_amdgcn_s_barrier();
          __builtin_amdgcn_sched_barrier(0);
        }
#pragma unroll
        for (int j = 0; j < 8; ++j) {
          float s = 0.f;
#pragma unroll
          for (int kq = 0; kq < 4; ++kq) {
            u64 g = gq[j * 4 + kq];
            s += __uint_as_float((unsigned)g); ok &= (g >> 32) == 1ull;
          }
          qv[j] = s;
        }
        if (__all(ok)) break;
        if (++spins > SPIN_CAP) break;
        __builtin_amdgcn_s_sleep(1);
      }
#pragma unroll
      for (int j = 0; j < 8; ++j) L.qs[j * 64 + lane] = rbf(qv[j]);
    }
    bar();
    if (hasF) {
      unsigned spins = 0;
      float s = 0.f;
      for (;;) {
        bool ok = true; s = 0.f;
#pragma unroll
        for (int p = 0; p < 2; ++p) {
          const int kq = p * 4 + (lane >> 4), vi = lane & 15;
          u64 g = gload(G + G_GU + (size_t)((b >> 3) * 8 + kq) * 128 + (vi >> 3) * 64 + (b & 7) * 8 + (vi & 7));
          s += __uint_as_float((unsigned)g); ok &= (g >> 32) == 1ull;
        }
        if (__all(ok)) break;
        if (++spins > SPIN_CAP) break;
        __builtin_amdgcn_s_sleep(1);
      }
      s = xsum16_32(s);
      float g = rbf(s), u = rbf(__shfl(s, (lane & 7) + 8, 64));
      float hv = rbf(rbf(1.0f / (1.0f + expf(-g))) * u);
      float acc[8] = {0, 0, 0, 0, 0, 0, 0, 0};
#pragma unroll
      for (int i = 0; i < 8; ++i) fma8(acc, wdv[i], __shfl(hv, i, 64));
      if (lane < 56) {
#pragma unroll
        for (int k = 0; k < 8; ++k) gstore(G + G_FR + (size_t)b * DM + lane * 8 + k, acc[k]);
      }
    }
    bar();
    bar();
    bar();
    if (!hasF) return;
    bar();
    bar();
    if (rg != 0) return;
    bar();
  }
}

extern "C" void kernel_launch(void* const* d_in, const int* in_sizes, int n_in,
                              void* d_out, int out_size, void* d_ws, size_t ws_size,
                              hipStream_t stream) {
  hipMemsetAsync(d_ws, 0, G_END * sizeof(u64), stream);
  fused_kernel<<<NWG, NTH, 0, stream>>>((const u16*)d_in[0], (const u16*)d_in[1], (const u16*)d_in[2],
                                        (const u16*)d_in[3], (const u16*)d_in[4], (const u16*)d_in[6],
                                        (const u16*)d_in[7], (const u16*)d_in[8], (u64*)d_ws, (u16*)d_out);
}
```

```cpp
#include <hip/hip_runtime.h>
#include <hip/hip_bf16.h>
#include <stdint.h>

typedef unsigned short u16;
typedef unsigned long long u64;
typedef __attribute__((ext_vector_type(4))) unsigned int u32x4;

constexpr int DM = 448, DH = 256, SK = 65536, WIN = 4096, FF = 1792;
constexpr int NCHUNK = 32, CHUNK = 128;
constexpr int NWG = 256, NTH = 576;
constexpr unsigned SPIN_CAP = 1u << 18;

constexpr size_t G_Q   = 0;
constexpr size_t G_GU  = G_Q + 64 * 4 * 64;
constexpr size_t G_ML  = G_GU + 28 * 8 * 128;
constexpr size_t G_PO  = G_ML + 256 * 4;
constexpr size_t G_FR  = G_PO + 256 * 512;
constexpr size_t G_AP  = G_FR + 224 * 448;
constexpr size_t G_FP  = G_AP + 32 * 448;
constexpr size_t G_END = G_FP + 32 * 448;

__device__ u64 G[G_END + 65536 + 256 * 32];
__device__ unsigned g_epoch[64];

__device__ __forceinline__ float bf2f(u16 v) { return __uint_as_float(((unsigned)v) << 16); }
__device__ __forceinline__ u16 f2bf(float f) {
  unsigned u = __float_as_uint(f);
  if ((u & 0x7fffffffu) > 0x7f800000u) return (u16)((u >> 16) | 0x40);
  u += 0x7fffu + ((u >> 16) & 1u);
  return (u16)(u >> 16);
}
__device__ __forceinline__ float rbf(float f) { return bf2f(f2bf(f)); }

template <int CTRL> __device__ __forceinline__ float dpp(float x) {
  return __builtin_bit_cast(float, __builtin_amdgcn_update_dpp(0, __builtin_bit_cast(int, x), CTRL, 0xf, 0xf, true));
}
__device__ __forceinline__ float xsum16_32(float x) {
  auto s = __builtin_amdgcn_permlane16_swap(__float_as_uint(x), __float_as_uint(x), false, false);
  x = __uint_as_float(s[0]) + __uint_as_float(s[1]);
  auto t = __builtin_amdgcn_permlane32_swap(__float_as_uint(x), __float_as_uint(x), false, false);
  return __uint_as_float(t[0]) + __uint_as_float(t[1]);
}
__device__ __forceinline__ float xmax16_32(float x) {
  auto s = __builtin_amdgcn_permlane16_swap(__float_as_uint(x), __float_as_uint(x), false, false);
  x = fmaxf(__uint_as_float(s[0]), __uint_as_float(s[1]));
  auto t = __builtin_amdgcn_permlane32_swap(__float_as_uint(x), __float_as_uint(x), false, false);
  return fmaxf(__uint_as_float(t[0]), __uint_as_float(t[1]));
}
__device__ __forceinline__ float wave_sum(float v) {
  v += dpp<0xB1>(v); v += dpp<0x4E>(v); v += dpp<0x141>(v); v += dpp<0x140>(v);
  return xsum16_32(v);
}
__device__ __forceinline__ float wave_max(float v) {
  v = fmaxf(v, dpp<0xB1>(v)); v = fmaxf(v, dpp<0x4E>(v)); v = fmaxf(v, dpp<0x141>(v)); v = fmaxf(v, dpp<0x140>(v));
  return xmax16_32(v);
}

#define gstore(p, v) __hip_atomic_store((p), (TAG << 32) | (u64)__float_as_uint(v), __ATOMIC_RELAXED, __HIP_MEMORY_SCOPE_AGENT)
__device__ __forceinline__ u64 gload(const u64* p) {
  return __hip_atomic_load(p, __ATOMIC_RELAXED, __HIP_MEMORY_SCOPE_AGENT);
}
__device__ __forceinline__ void bar() {
  asm volatile("s_waitcnt lgkmcnt(0)" ::: "memory");
  __builtin_amdgcn_s_barrier();
  asm volatile("" ::: "memory");
}
__device__ __forceinline__ void fma8(float (&acc)[8], u32x4 w, float s) {
#pragma unroll
  for (int k = 0; k < 4; ++k) {
    acc[2 * k]     = fmaf(s, __uint_as_float(w[k] << 16), acc[2 * k]);
    acc[2 * k + 1] = fmaf(s, __uint_as_float(w[k] & 0xffff0000u), acc[2 * k + 1]);
  }
}
__device__ __forceinline__ u32x4 ldnt(const u16* p) { return __builtin_nontemporal_load((const u32x4*)p); }

struct LdsT {
  float xn[DM];
  float qred[8][64];
  float gred[8][128];
  float qs[512];
  float sc[2][CHUNK];
  float ored[8][2][DH];
};

__global__ void __launch_bounds__(NTH, 1)
fused_kernel(const u16* __restrict__ x, const u16* __restrict__ kc, const u16* __restrict__ vc,
             const u16* __restrict__ wq, const u16* __restrict__ wo, const u16* __restrict__ wg,
             const u16* __restrict__ wu, const u16* __restrict__ wd,
             u16* __restrict__ out) {
  __shared__ LdsT L;
  const unsigned epoch = __hip_atomic_load(&g_epoch[0], __ATOMIC_RELAXED, __HIP_MEMORY_SCOPE_AGENT);
  const u64 TAG = (u64)(epoch + 1u);
  const int t = threadIdx.x, b = blockIdx.x, lane = t & 63, wave = t >> 6;
  const int kvh = b >> 5, ch = b & 31;
  const bool hasF = b < 224;
  const int cg = b % 7, rg = b / 7;

  if (wave < 8) {
    u32x4 xv = *(const u32x4*)(x + min(lane, 55) * 8);
    __builtin_amdgcn_sched_barrier(0);
    const int r8 = t >> 3;
    const int qrow = (b & 3) * 112 + r8, qcol = (b >> 2) * 64 + (t & 7) * 8;
    const int qrow1 = (r8 < 48) ? qrow + 64 : qrow;
    u32x4 wq0 = ldnt(wq + (size_t)qrow * 4096 + qcol);
    u32x4 wq1 = ldnt(wq + (size_t)qrow1 * 4096 + qcol);
    const int frow = (b & 7) * 56 + min(r8, 55), fcol = (hasF ? (b >> 3) * 64 : 0) + (t & 7) * 8;
    u32x4 wgv = ldnt(wg + (size_t)frow * FF + fcol), wuv = ldnt(wu + (size_t)frow * FF + fcol);
    const int half = lane >> 5, d8 = (lane & 31) * 8;
    const size_t base = ((size_t)kvh * SK + (SK - WIN) + (size_t)ch * CHUNK + wave * 16 + half) * DH + d8;
    u32x4 K[8], V[8];
    __builtin_amdgcn_sched_barrier(0);
    __builtin_amdgcn_s_barrier();
    __builtin_amdgcn_sched_barrier(0);
#pragma unroll
    for (int i = 0; i < 8; ++i) K[i] = ldnt(kc + base + (size_t)i * 2 * DH);
#pragma unroll
    for (int i = 0; i < 8; ++i) V[i] = ldnt(vc + base + (size_t)i * 2 * DH);
    u32x4 wov[2] = {{0, 0, 0, 0}, {0, 0, 0, 0}};
    if (hasF) {
      const u16* wp = wo + (size_t)(rg * 128 + (t >> 3)) * DM + cg * 64 + (t & 7) * 8;
      wov[0] = ldnt(wp); wov[1] = ldnt(wp + (size_t)64 * DM);
    }
    __builtin_amdgcn_sched_barrier(0);
    {
      float xf[8];
      __builtin_amdgcn_sched_barrier(0);
      if (lane >= 56) xv = u32x4{0, 0, 0, 0};
#pragma unroll
      for (int k = 0; k < 4; ++k) { xf[2 * k] = __uint_as_float(xv[k] << 16); xf[2 * k + 1] = __uint_as_float(xv[k] & 0xffff0000u); }
      float s = 0.f;
#pragma unroll
      for (int k = 0; k < 8; ++k) s += xf[k];
      float mean = wave_sum(s) * (1.0f / DM);
      float v = 0.f;
      if (lane < 56) {
#pragma unroll
        for (int k = 0; k < 8; ++k) { float d = xf[k] - mean; v = fmaf(d, d, v); }
      }
      float var = wave_sum(v) * (1.0f / DM);
      float mb = rbf(mean), vb = rbf(var);
      float den = rbf(sqrtf(rbf(vb + 1e-5f)));
      if (lane < 56) {
#pragma unroll
        for (int k = 0; k < 8; ++k) L.xn[lane * 8 + k] = rbf(rbf(xf[k] - mb) / den);
      }
      asm volatile("s_waitcnt lgkmcnt(0)" ::: "memory");
    }
    {
      float acc[8] = {0, 0, 0, 0, 0, 0, 0, 0};
      fma8(acc, wq0, L.xn[qrow]);
      fma8(acc, wq1, (r8 < 48) ? L.xn[qrow1] : 0.f);
      float ag[8] = {0, 0, 0, 0, 0, 0, 0, 0}, au[8] = {0, 0, 0, 0, 0, 0, 0, 0};
      { float s = (r8 < 56) ? L.xn[frow] : 0.f; fma8(ag, wgv, s); fma8(au, wuv, s); }
#pragma unroll
      for (int k = 0; k < 8; ++k) {
        acc[k] = xsum16_32(acc[k] + dpp<0x128>(acc[k]));
        ag[k] = xsum16_32(ag[k] + dpp<0x128>(ag[k]));
        au[k] = xsum16_32(au[k] + dpp<0x128>(au[k]));
      }
      if (lane < 8) {
#pragma unroll
        for (int k = 0; k < 8; ++k) {
          L.qred[wave][lane * 8 + k] = acc[k];
          L.gred[wave][lane * 8 + k] = ag[k]; L.gred[wave][64 + lane * 8 + k] = au[k];
        }
      }
    }
    bar();
    bar();
    float q0[8], q1[8];
#pragma unroll
    for (int k = 0; k < 8; ++k) { q0[k] = L.qs[d8 + k]; q1[k] = L.qs[256 + d8 + k]; }
    float sv[16];
#pragma unroll
    for (int i = 0; i < 8; ++i) {
      float s0 = 0.f, s1 = 0.f;
#pragma unroll
      for (int k = 0; k < 4; ++k) {
        float lo = __uint_as_float(K[i][k] << 16), hi = __uint_as_float(K[i][k] & 0xffff0000u);
        s0 = fmaf(lo, q0[2 * k], s0); s0 = fmaf(hi, q0[2 * k + 1], s0);
        s1 = fmaf(lo, q1[2 * k], s1); s1 = fmaf(hi, q1[2 * k + 1], s1);
      }
      sv[2 * i] = s0; sv[2 * i + 1] = s1;
    }
#pragma unroll
    for (int st = 0; st < 4; ++st) {
      const int n = 8 >> st, bit = 16 >> st;
#pragma unroll
      for (int j = 0; j < n; ++j) {
        float lo = sv[j], hi = sv[j + n];
        float keep = (lane & bit) ? hi : lo, give = (lane & bit) ? lo : hi;
        sv[j] = keep + __shfl_xor(give, bit, 64);
      }
    }
    {
      float s = sv[0] + __shfl_xor(sv[0], 1, 64);
      int vi = ((lane >> 4) & 1) * 8 + ((lane >> 3) & 1) * 4 + ((lane >> 2) & 1) * 2 + ((lane >> 1) & 1);
      int row = wave * 16 + (vi >> 1) * 2 + half, hh = vi & 1;
      if ((lane & 1) == 0) L.sc[hh][row] = rbf(s) * 0.0625f;
    }
    bar();
    float m0, m1, l0, l1;
    {
      float a0 = L.sc[0][lane], a1 = L.sc[0][lane + 64], c0 = L.sc[1][lane], c1 = L.sc[1][lane + 64];
      m0 = wave_max(fmaxf(a0, a1)); m1 = wave_max(fmaxf(c0, c1));
      l0 = wave_sum(__expf(a0 - m0) + __expf(a1 - m0));
      l1 = wave_sum(__expf(c0 - m1) + __expf(c1 - m1));
    }
    float a0[8] = {0, 0, 0, 0, 0, 0, 0, 0}, a1[8] = {0, 0, 0, 0, 0, 0, 0, 0};
#pragma unroll
    for (int i = 0; i < 8; ++i) {
      int row = wave * 16 + 2 * i + half;
      float p0 = __expf(L.sc[0][row] - m0), p1 = __expf(L.sc[1][row] - m1);
      fma8(a0, V[i], p0);
      fma8(a1, V[i], p1);
    }
#pragma unroll
    for (int k = 0; k < 8; ++k) { a0[k] += __shfl_xor(a0[k], 32, 64); a1[k] += __shfl_xor(a1[k], 32, 64); }
    if (lane < 32) {
#pragma unroll
      for (int k = 0; k < 8; ++k) { L.ored[wave][0][d8 + k] = a0[k]; L.ored[wave][1][d8 + k] = a1[k]; }
    }
    bar();
    {
      const float* o = &L.ored[0][0][0];
      float s = 0.f;
#pragma unroll
      for (int w = 0; w < 8; ++w) s += o[w * 512 + t];
      gstore(G + G_PO + (size_t)b * 512 + t, s);
      if (t < 4) gstore(G + G_ML + b * 4 + t, t == 0 ? m0 : t == 1 ? l0 : t == 2 ? m1 : l1);
    }
    bar();
    if (!hasF) return;
    const int h = rg >> 1, kv3 = h >> 1, hh = h & 1, d0 = (rg & 1) * 128;
    float wgt8[8];
    {
      const int d = t & 127, cq = t >> 7;
      float ov[8], mm = 0.f, ll = 0.f;
      unsigned spins = 0;
      for (;;) {
        bool ok = true;
        u64 gm = (TAG << 32), gl = (TAG << 32);
        if (lane < 32) { gm = gload(G + G_ML + (kv3 * 32 + lane) * 4 + hh * 2); gl = gload(G + G_ML + (kv3 * 32 + lane) * 4 + hh * 2 + 1); }
#pragma unroll
        for (int j = 0; j < 8; ++j) {
          u64 g = gload(G + G_PO + (size_t)(kv3 * 32 + cq * 8 + j) * 512 + hh * 256 + d0 + d);
          ov[j] = __uint_as_float((unsigned)g); ok &= (g >> 32) == TAG;
        }
        ok &= ((gm >> 32) == TAG) && ((gl >> 32) == TAG);
        mm = __uint_as_float((unsigned)gm); ll = __uint_as_float((unsigned)gl);
        if (__all(ok)) break;
        if (++spins > SPIN_CAP) break;
      }
      if (lane >= 32) { mm = -INFINITY; ll = 0.f; }
      float M = wave_max(mm);
      float e = (lane < 32) ? __expf(mm - M) : 0.f;
      float Lsum = wave_sum(e * ll);
      float wv = e / Lsum;
      float s = 0.f;
#pragma unroll
      for (int j = 0; j < 8; ++j) s = fmaf(__shfl(wv, cq * 8 + j, 64), ov[j], s);
      float* fr = &L.ored[0][0][0];
      fr[t] = s;
      (void)wgt8;
    }
    bar();
    {
      const float* fr = &L.ored[0][0][0];
      const int r = t >> 3;
      float o0 = rbf(fr[r] + fr[128 + r] + fr[256 + r] + fr[384 + r]);
      float o1 = rbf(fr[64 + r] + fr[192 + r] + fr[320 + r] + fr[448 + r]);
      float acc[8] = {0, 0, 0, 0, 0, 0, 0, 0};
      fma8(acc, wov[0], o0); fma8(acc, wov[1], o1);
#pragma unroll
      for (int k = 0; k < 8; ++k) {
        float v = acc[k];
        v += __shfl_xor(v, 8, 64); v += __shfl_xor(v, 16, 64); v += __shfl_xor(v, 32, 64);
        acc[k] = v;
      }
      float* red = &L.ored[2][0][0];
      if (lane < 8) {
#pragma unroll
        for (int k = 0; k < 8; ++k) red[wave * 64 + lane * 8 + k] = acc[k];
      }
    }
    bar();
    if (wave == 0) {
      const float* red = &L.ored[2][0][0];
      float a = 0.f;
#pragma unroll
      for (int w = 0; w < 8; ++w) a += red[w * 64 + lane];
      float f = 0.f;
      unsigned spins = 0;
      for (;;) {
        bool ok = true; f = 0.f;
#pragma unroll
        for (int j = 0; j < 7; ++j) {
          u64 g = gload(G + G_FR + (size_t)(rg * 7 + j) * DM + cg * 64 + lane);
          f += __uint_as_float((unsigned)g); ok &= (g >> 32) == TAG;
        }
        if (__all(ok)) break;
        if (++spins > SPIN_CAP) break;
      }
      gstore(G + G_AP + rg * DM + cg * 64 + lane, a);
      gstore(G + G_FP + rg * DM + cg * 64 + lane, f);
    }
    if (rg != 0) return;
    {
      float a = 0.f, f = 0.f;
      unsigned spins = 0;
      for (;;) {
        bool ok = true; a = 0.f; f = 0.f;
#pragma unroll
        for (int j = 0; j < 4; ++j) {
          u64 ga = gload(G + G_AP + (wave * 4 + j) * DM + cg * 64 + lane);
          u64 gf = gload(G + G_FP + (wave * 4 + j) * DM + cg * 64 + lane);
          a += __uint_as_float((unsigned)ga); f += __uint_as_float((unsigned)gf);
          ok &= ((ga >> 32) == TAG) && ((gf >> 32) == TAG);
        }
        if (__all(ok)) break;
        if (++spins > SPIN_CAP) break;
      }
      float* red = &L.ored[4][0][0];
      red[wave * 128 + lane] = a; red[wave * 128 + 64 + lane] = f;
    }
    bar();
    if (wave == 0) {
      const float* red = &L.ored[4][0][0];
      float a = 0.f, f = 0.f;
#pragma unroll
      for (int w = 0; w < 8; ++w) { a += red[w * 128 + lane]; f += red[w * 128 + 64 + lane]; }
      const int c = cg * 64 + lane;
      float xo = bf2f(x[c]);
      out[c] = f2bf(rbf(xo + rbf(a)) + rbf(f));
      if (b == 0 && lane == 0) __hip_atomic_store(&g_epoch[0], epoch + 1u, __ATOMIC_RELAXED, __HIP_MEMORY_SCOPE_AGENT);
    }
  } else {
    u32x4 wdv[8];
#pragma unroll
    for (int i = 0; i < 8; ++i) wdv[i] = u32x4{0, 0, 0, 0};
    if (hasF && lane < 56) {
#pragma unroll
      for (int i = 0; i < 8; ++i) wdv[i] = ldnt(wd + (size_t)(b * 8 + i) * DM + lane * 8);
    }
    __builtin_amdgcn_sched_barrier(0);
    __builtin_amdgcn_s_barrier();
    __builtin_amdgcn_sched_barrier(0);
    bar();
    {
      float v = 0.f, g = 0.f, u = 0.f;
#pragma unroll
      for (int w = 0; w < 8; ++w) { v += L.qred[w][lane]; g += L.gred[w][lane]; u += L.gred[w][64 + lane]; }
      gstore(G + G_Q + (size_t)b * 64 + lane, v);
      if (hasF) { gstore(G + G_GU + (size_t)b * 128 + lane, g); gstore(G + G_GU + (size_t)b * 128 + 64 + lane, u); }
    }
    {
      unsigned spins = 0;
      float qv[8];
      for (;;) {
        bool ok = true;
        u64 gq[32];
#pragma unroll
        for (int j = 0; j < 32; ++j) gq[j] = gload(G + G_Q + (size_t)(kvh * 32 + j) * 64 + lane);
#pragma unroll
        for (int j = 0; j < 8; ++j) {
          float s = 0.f;
#pragma unroll
          for (int kq = 0; kq < 4; ++kq) {
            u64 g = gq[j * 4 + kq];
            s += __uint_as_float((unsigned)g); ok &= (g >> 32) == TAG;
          }
          qv[j] = s;
        }
        if (__all(ok)) break;
        if (++spins > SPIN_CAP) break;
      }
#pragma unroll
      for (int j = 0; j < 8; ++j) L.qs[j * 64 + lane] = rbf(qv[j]);
    }
    bar();
    if (hasF) {
      unsigned spins = 0;
      float s = 0.f;
      for (;;) {
        bool ok = true; s = 0.f;
#pragma unroll
        for (int p = 0; p < 2; ++p) {
          const int kq = p * 4 + (lane >> 4), vi = lane & 15;
          u64 g = gload(G + G_GU + (size_t)((b >> 3) * 8 + kq) * 128 + (vi >> 3) * 64 + (b & 7) * 8 + (vi & 7));
          s += __uint_as_float((unsigned)g); ok &= (g >> 32) == TAG;
        }
        if (__all(ok)) break;
        if (++spins > SPIN_CAP) break;
      }
      s = xsum16_32(s);
      float g = rbf(s), u = rbf(__shfl(s, (lane & 7) + 8, 64));
      float hv = rbf(rbf(1.0f / (1.0f + expf(-g))) * u);
      float acc[8] = {0, 0, 0, 0, 0, 0, 0, 0};
#pragma unroll
      for (int i = 0; i < 8; ++i) fma8(acc, wdv[i], __shfl(hv, i, 64));
      if (lane < 56) {
#pragma unroll
        for (int k = 0; k < 8; ++k) gstore(G + G_FR + (size_t)b * DM + lane * 8 + k, acc[k]);
      }
    }
    bar();
    bar();
    bar();
    if (!hasF) return;
    bar();
    bar();
    if (rg != 0) return;
    bar();
  }
}

extern "C" void kernel_launch(void* const* d_in, const int* in_sizes, int n_in,
                              void* d_out, int out_size, void* d_ws, size_t ws_size,
                              hipStream_t stream) {
  fused_kernel<<<NWG, NTH, 0, stream>>>((const u16*)d_in[0], (const u16*)d_in[1], (const u16*)d_in[2],
                                        (const u16*)d_in[3], (const u16*)d_in[4], (const u16*)d_in[6],
                                        (const u16*)d_in[7], (const u16*)d_in[8], (u16*)d_out);
}
```

```cpp
#include <hip/hip_runtime.h>
#include <hip/hip_bf16.h>
#include <stdint.h>

typedef unsigned short u16;
typedef unsigned long long u64;
typedef __attribute__((ext_vector_type(4))) unsigned int u32x4;
typedef __attribute__((ext_vector_type(4))) float f32x4;

constexpr int DM = 448, DH = 256, SK = 65536, WIN = 4096, FF = 1792;
constexpr int NCHUNK = 32, CHUNK = 128;
#ifndef NV0
#define NV0 2
#endif
constexpr int NWG = 256, NTH = 576;
constexpr unsigned SPIN_CAP = 1u << 18;

constexpr size_t G_Q   = 0;
constexpr size_t G_GU  = G_Q + 64 * 4 * 64;
constexpr size_t G_ML  = G_GU + 28 * 8 * 128;
constexpr size_t G_PO  = G_ML + 256 * 4;
constexpr size_t G_FR  = G_PO + 256 * 512;
constexpr size_t G_AP  = G_FR + 224 * 448;
constexpr size_t G_FP  = G_AP + 32 * 448;
constexpr size_t G_END = G_FP + 32 * 448;

__device__ u64 G[G_END + 65536 + 256 * 32];
__device__ unsigned g_epoch[64];

__device__ __forceinline__ float bf2f(u16 v) { return __uint_as_float(((unsigned)v) << 16); }
__device__ __forceinline__ u16 f2bf(float f) {
  unsigned u = __float_as_uint(f);
  if ((u & 0x7fffffffu) > 0x7f800000u) return (u16)((u >> 16) | 0x40);
  u += 0x7fffu + ((u >> 16) & 1u);
  return (u16)(u >> 16);
}
__device__ __forceinline__ unsigned cvtpk(float lo, float hi) {
  unsigned r; asm("v_cvt_pk_bf16_f32 %0, %1, %2" : "=v"(r) : "v"(lo), "v"(hi)); return r;
}
__device__ __forceinline__ float rbf(float f) { return __uint_as_float(cvtpk(f, f) << 16); }
typedef __attribute__((ext_vector_type(2))) __bf16 bf16x2;
typedef __attribute__((ext_vector_type(2))) float f32x2;
__device__ __forceinline__ float dot2(unsigned a, unsigned b, float acc) {
  return __builtin_amdgcn_fdot2_f32_bf16(__builtin_bit_cast(bf16x2, a), __builtin_bit_cast(bf16x2, b), acc, false);
}

template <int CTRL> __device__ __forceinline__ float dpp(float x) {
  return __builtin_bit_cast(float, __builtin_amdgcn_update_dpp(0, __builtin_bit_cast(int, x), CTRL, 0xf, 0xf, true));
}
__device__ __forceinline__ float xsum16_32(float x) {
  auto s = __builtin_amdgcn_permlane16_swap(__float_as_uint(x), __float_as_uint(x), false, false);
  x = __uint_as_float(s[0]) + __uint_as_float(s[1]);
  auto t = __builtin_amdgcn_permlane32_swap(__float_as_uint(x), __float_as_uint(x), false, false);
  return __uint_as_float(t[0]) + __uint_as_float(t[1]);
}
__device__ __forceinline__ float xmax16_32(float x) {
  auto s = __builtin_amdgcn_permlane16_swap(__float_as_uint(x), __float_as_uint(x), false, false);
  x = fmaxf(__uint_as_float(s[0]), __uint_as_float(s[1]));
  auto t = __builtin_amdgcn_permlane32_swap(__float_as_uint(x), __float_as_uint(x), false, false);
  return fmaxf(__uint_as_float(t[0]), __uint_as_float(t[1]));
}
__device__ __forceinline__ float wave_sum(float v) {
  v += dpp<0xB1>(v); v += dpp<0x4E>(v); v += dpp<0x141>(v); v += dpp<0x140>(v);
  return xsum16_32(v);
}
__device__ __forceinline__ float wave_max(float v) {
  v = fmaxf(v, dpp<0xB1>(v)); v = fmaxf(v, dpp<0x4E>(v)); v = fmaxf(v, dpp<0x141>(v)); v = fmaxf(v, dpp<0x140>(v));
  return xmax16_32(v);
}

#define gstore(p, v) __hip_atomic_store((p), (TAG << 32) | (u64)__float_as_uint(v), __ATOMIC_RELAXED, __HIP_MEMORY_SCOPE_AGENT)
__device__ __forceinline__ u64 gload(const u64* p) {
  return __hip_atomic_load(p, __ATOMIC_RELAXED, __HIP_MEMORY_SCOPE_AGENT);
}
__device__ __forceinline__ void bar() {
  asm volatile("s_waitcnt lgkmcnt(0)" ::: "memory");
  __builtin_amdgcn_s_barrier();
  asm volatile("" ::: "memory");
}
__device__ __forceinline__ void fma8(float (&acc)[8], u32x4 w, float s) {
#pragma unroll
  for (int k = 0; k < 4; ++k) {
    acc[2 * k]     = fmaf(s, __uint_as_float(w[k] << 16), acc[2 * k]);
    acc[2 * k + 1] = fmaf(s, __uint_as_float(w[k] & 0xffff0000u), acc[2 * k + 1]);
  }
}
__device__ __forceinline__ u32x4 ldnt(const u16* p) { return __builtin_nontemporal_load((const u32x4*)p); }

struct LdsT {
  float xn[DM];
  float qred[8][64];
  float gred[8][128];
  unsigned qs[256];
  float sc[2][CHUNK];
  float ored[8][2][DH];
};

__global__ void __launch_bounds__(NTH, 1)
fused_kernel(const u16* __restrict__ x, const u16* __restrict__ kc, const u16* __restrict__ vc,
             const u16* __restrict__ wq, const u16* __restrict__ wo, const u16* __restrict__ wg,
             const u16* __restrict__ wu, const u16* __restrict__ wd,
             u16* __restrict__ out) {
  __shared__ LdsT L;
  const unsigned epoch = __hip_atomic_load(&g_epoch[0], __ATOMIC_RELAXED, __HIP_MEMORY_SCOPE_AGENT);
  const u64 TAG = (u64)(epoch + 1u);
  const int t = threadIdx.x, b = blockIdx.x, lane = t & 63, wave = t >> 6;
  const int kvh = b >> 5, ch = b & 31;
  const bool hasF = b < 224;
  const int cg = b % 7, rg = b / 7;

  if (wave < 8) {

    const int r8 = t >> 3;
    const int qrow = (b & 3) * 112 + r8, qcol = (b >> 2) * 64 + (t & 7) * 8;
    const int qrow1 = (r8 < 48) ? qrow + 64 : qrow;
    u32x4 wq0 = ldnt(wq + (size_t)qrow * 4096 + qcol);
    u32x4 wq1 = ldnt(wq + (size_t)qrow1 * 4096 + qcol);
    const int frow = (b & 7) * 56 + min(r8, 55), fcol = (hasF ? (b >> 3) * 64 : 0) + (t & 7) * 8;
    u32x4 wgv = ldnt(wg + (size_t)frow * FF + fcol), wuv = ldnt(wu + (size_t)frow * FF + fcol);
    const int half = lane >> 5, d8 = (lane & 31) * 8;
    const size_t base = ((size_t)kvh * SK + (SK - WIN) + (size_t)ch * CHUNK + wave * 16 + half) * DH + d8;
    u32x4 K[8], V[8];
    __builtin_amdgcn_sched_barrier(0);
    __builtin_amdgcn_s_barrier();
    __builtin_amdgcn_sched_barrier(0);
#pragma unroll
    for (int i = 0; i < 8; ++i) K[i] = ldnt(kc + base + (size_t)i * 2 * DH);
#pragma unroll
    for (int i = 0; i < NV0; ++i) V[i] = ldnt(vc + base + (size_t)i * 2 * DH);
    __builtin_amdgcn_sched_barrier(0);
    bar();
    {
      float acc[8] = {0, 0, 0, 0, 0, 0, 0, 0};
      fma8(acc, wq0, L.xn[qrow]);
      fma8(acc, wq1, (r8 < 48) ? L.xn[qrow1] : 0.f);
      float ag[8] = {0, 0, 0, 0, 0, 0, 0, 0}, au[8] = {0, 0, 0, 0, 0, 0, 0, 0};
      { float s = (r8 < 56) ? L.xn[frow] : 0.f; fma8(ag, wgv, s); fma8(au, wuv, s); }
#pragma unroll
      for (int k = 0; k < 8; ++k) {
        acc[k] = xsum16_32(acc[k] + dpp<0x128>(acc[k]));
        ag[k] = xsum16_32(ag[k] + dpp<0x128>(ag[k]));
        au[k] = xsum16_32(au[k] + dpp<0x128>(au[k]));
      }
      if (lane < 8) {
#pragma unroll
        for (int k = 0; k < 8; ++k) {
          L.qred[wave][lane * 8 + k] = acc[k];
          L.gred[wave][lane * 8 + k] = ag[k]; L.gred[wave][64 + lane * 8 + k] = au[k];
        }
      }
    }
    bar();
    bar();
    __builtin_amdgcn_sched_barrier(0);
#pragma unroll
    for (int i = NV0; i < 8; ++i) V[i] = ldnt(vc + base + (size_t)i * 2 * DH);
    u32x4 wov[2] = {{0, 0, 0, 0}, {0, 0, 0, 0}};
    if (hasF) {
      const u16* wp = wo + (size_t)(rg * 128 + (t >> 3)) * DM + cg * 64 + (t & 7) * 8;
      wov[0] = ldnt(wp); wov[1] = ldnt(wp + (size_t)64 * DM);
    }
    __builtin_amdgcn_sched_barrier(0);
    const u32x4 q0 = *(const u32x4*)&L.qs[d8 >> 1], q1 = *(const u32x4*)&L.qs[128 + (d8 >> 1)];
    float sv[16];
#pragma unroll
    for (int i = 0; i < 8; ++i) {
      float s0 = dot2(K[i][0], q0[0], 0.f), s1 = dot2(K[i][0], q1[0], 0.f);
      float t0 = dot2(K[i][1], q0[1], 0.f), t1 = dot2(K[i][1], q1[1], 0.f);
      s0 = dot2(K[i][2], q0[2], s0); s1 = dot2(K[i][2], q1[2], s1);
      t0 = dot2(K[i][3], q0[3], t0); t1 = dot2(K[i][3], q1[3], t1);
      sv[2 * i] = s0 + t0; sv[2 * i + 1] = s1 + t1;
    }
#pragma unroll
    for (int st = 0; st < 4; ++st) {
      const int n = 8 >> st, bit = 16 >> st;
#pragma unroll
      for (int j = 0; j < n; ++j) {
        float lo = sv[j], hi = sv[j + n];
        float keep = (lane & bit) ? hi : lo, give = (lane & bit) ? lo : hi;
        sv[j] = keep + __shfl_xor(give, bit, 64);
      }
    }
    {
      float s = sv[0] + __shfl_xor(sv[0], 1, 64);
      int vi = ((lane >> 4) & 1) * 8 + ((lane >> 3) & 1) * 4 + ((lane >> 2) & 1) * 2 + ((lane >> 1) & 1);
      int row = wave * 16 + (vi >> 1) * 2 + half, hh = vi & 1;
      if ((lane & 1) == 0) L.sc[hh][row] = rbf(s) * 0.0625f;
    }
    bar();
    float m0, m1, l0, l1;
    {
      float a0 = L.sc[0][lane], a1 = L.sc[0][lane + 64], c0 = L.sc[1][lane], c1 = L.sc[1][lane + 64];
      m0 = wave_max(fmaxf(a0, a1)); m1 = wave_max(fmaxf(c0, c1));
      l0 = wave_sum(__expf(a0 - m0) + __expf(a1 - m0));
      l1 = wave_sum(__expf(c0 - m1) + __expf(c1 - m1));
    }
    f32x2 A0[4], A1[4];
#pragma unroll
    for (int k = 0; k < 4; ++k) { A0[k] = f32x2{0.f, 0.f}; A1[k] = f32x2{0.f, 0.f}; }
#pragma unroll
    for (int i = 0; i < 8; ++i) {
      int row = wave * 16 + 2 * i + half;
      float p0 = __expf(L.sc[0][row] - m0), p1 = __expf(L.sc[1][row] - m1);
      const f32x2 P0 = {p0, p0}, P1 = {p1, p1};
#pragma unroll
      for (int k = 0; k < 4; ++k) {
        f32x2 v = {__uint_as_float(V[i][k] << 16), __uint_as_float(V[i][k] & 0xffff0000u)};
        A0[k] = __builtin_elementwise_fma(v, P0, A0[k]);
        A1[k] = __builtin_elementwise_fma(v, P1, A1[k]);
      }
    }
    float a0[8], a1[8];
#pragma unroll
    for (int k = 0; k < 4; ++k) { a0[2 * k] = A0[k][0]; a0[2 * k + 1] = A0[k][1]; a1[2 * k] = A1[k][0]; a1[2 * k + 1] = A1[k][1]; }
#pragma unroll
    for (int k = 0; k < 8; ++k) {
      auto tt = __builtin_amdgcn_permlane32_swap(__float_as_uint(a0[k]), __float_as_uint(a1[k]), false, false);
      a0[k] = __uint_as_float(tt[0]) + __uint_as_float(tt[1]);
    }
    {
      float* dst = &L.ored[wave][half][d8];
      *(f32x4*)dst = f32x4{a0[0], a0[1], a0[2], a0[3]};
      *(f32x4*)(dst + 4) = f32x4{a0[4], a0[5], a0[6], a0[7]};
    }
    bar();
    {
      const float* o = &L.ored[0][0][0];
      float s = 0.f;
#pragma unroll
      for (int w = 0; w < 8; ++w) s += o[w * 512 + t];
      gstore(G + G_PO + (size_t)b * 512 + t, s);
      if (t < 4) gstore(G + G_ML + b * 4 + t, t == 0 ? m0 : t == 1 ? l0 : t == 2 ? m1 : l1);
    }
    bar();
    if (!hasF) return;
    const int h = rg >> 1, kv3 = h >> 1, hh = h & 1, d0 = (rg & 1) * 128;
    if (wave == 0) {
      unsigned spins = 0;
      for (;;) {
        u64 g = gload(G + G_ML + (kv3 * 32 + (lane >> 1)) * 4 + hh * 2 + (lane & 1));
        if (__all((g >> 32) == TAG)) break;
        if (++spins > SPIN_CAP) break;
        __builtin_amdgcn_s_sleep(1);
      }
    }
    bar();
    float wgt8[8];
    {
      const int d = t & 127, cq = t >> 7;
      float ov[8], mm = 0.f, ll = 0.f;
      unsigned spins = 0;
      for (;;) {
        bool ok = true;
        u64 gm = (TAG << 32), gl = (TAG << 32);
        if (lane < 32) { gm = gload(G + G_ML + (kv3 * 32 + lane) * 4 + hh * 2); gl = gload(G + G_ML + (kv3 * 32 + lane) * 4 + hh * 2 + 1); }
#pragma unroll
        for (int j = 0; j < 8; ++j) {
          u64 g = gload(G + G_PO + (size_t)(kv3 * 32 + cq * 8 + j) * 512 + hh * 256 + d0 + d);
          ov[j] = __uint_as_float((unsigned)g); ok &= (g >> 32) == TAG;
        }
        ok &= ((gm >> 32) == TAG) && ((gl >> 32) == TAG);
        mm = __uint_as_float((unsigned)gm); ll = __uint_as_float((unsigned)gl);
        if (__all(ok)) break;
        if (++spins > SPIN_CAP) break;
      }
      if (lane >= 32) { mm = -INFINITY; ll = 0.f; }
      float M = wave_max(mm);
      float e = (lane < 32) ? __expf(mm - M) : 0.f;
      float Lsum = wave_sum(e * ll);
      float wv = e / Lsum;
      float s = 0.f;
#pragma unroll
      for (int j = 0; j < 8; ++j) s = fmaf(__shfl(wv, cq * 8 + j, 64), ov[j], s);
      float* fr = &L.ored[0][0][0];
      fr[t] = s;
      (void)wgt8;
    }
    bar();
    {
      const float* fr = &L.ored[0][0][0];
      const int r = t >> 3;
      float o0 = rbf(fr[r] + fr[128 + r] + fr[256 + r] + fr[384 + r]);
      float o1 = rbf(fr[64 + r] + fr[192 + r] + fr[320 + r] + fr[448 + r]);
      float acc[8] = {0, 0, 0, 0, 0, 0, 0, 0};
      fma8(acc, wov[0], o0); fma8(acc, wov[1], o1);
#pragma unroll
      for (int k = 0; k < 8; ++k) {
        float v = acc[k];
        v += __shfl_xor(v, 8, 64); v += __shfl_xor(v, 16, 64); v += __shfl_xor(v, 32, 64);
        acc[k] = v;
      }
      float* red = &L.ored[2][0][0];
      if (lane < 8) {
#pragma unroll
        for (int k = 0; k < 8; ++k) red[wave * 64 + lane * 8 + k] = acc[k];
      }
    }
    bar();
    if (wave == 0) {
      const float* red = &L.ored[2][0][0];
      float a = 0.f;
#pragma unroll
      for (int w = 0; w < 8; ++w) a += red[w * 64 + lane];
      float f = 0.f;
      unsigned spins = 0;
      for (;;) {
        bool ok = true; f = 0.f;
#pragma unroll
        for (int j = 0; j < 7; ++j) {
          u64 g = gload(G + G_FR + (size_t)(rg * 7 + j) * DM + cg * 64 + lane);
          f += __uint_as_float((unsigned)g); ok &= (g >> 32) == TAG;
        }
        if (__all(ok)) break;
        if (++spins > SPIN_CAP) break;
      }
      gstore(G + G_AP + rg * DM + cg * 64 + lane, a);
      gstore(G + G_FP + rg * DM + cg * 64 + lane, f);
    }
    if (rg != 0) return;
    {
      float a = 0.f, f = 0.f;
      unsigned spins = 0;
      for (;;) {
        bool ok = true; a = 0.f; f = 0.f;
#pragma unroll
        for (int j = 0; j < 4; ++j) {
          u64 ga = gload(G + G_AP + (wave * 4 + j) * DM + cg * 64 + lane);
          u64 gf = gload(G + G_FP + (wave * 4 + j) * DM + cg * 64 + lane);
          a += __uint_as_float((unsigned)ga); f += __uint_as_float((unsigned)gf);
          ok &= ((ga >> 32) == TAG) && ((gf >> 32) == TAG);
        }
        if (__all(ok)) break;
        if (++spins > SPIN_CAP) break;
      }
      float* red = &L.ored[4][0][0];
      red[wave * 128 + lane] = a; red[wave * 128 + 64 + lane] = f;
    }
    bar();
    if (wave == 0) {
      const float* red = &L.ored[4][0][0];
      float a = 0.f, f = 0.f;
#pragma unroll
      for (int w = 0; w < 8; ++w) { a += red[w * 128 + lane]; f += red[w * 128 + 64 + lane]; }
      const int c = cg * 64 + lane;
      float xo = bf2f(x[c]);
      out[c] = f2bf(rbf(xo + rbf(a)) + rbf(f));
      if (b == 0 && lane == 0) __hip_atomic_store(&g_epoch[0], epoch + 1u, __ATOMIC_RELAXED, __HIP_MEMORY_SCOPE_AGENT);
    }
  } else {
    u32x4 xv = *(const u32x4*)(x + min(lane, 55) * 8);
    __builtin_amdgcn_sched_barrier(0);
    u32x4 wdv[8];
#pragma unroll
    for (int i = 0; i < 8; ++i) wdv[i] = u32x4{0, 0, 0, 0};
    if (hasF && lane < 56) {
#pragma unroll
      for (int i = 0; i < 8; ++i) wdv[i] = ldnt(wd + (size_t)(b * 8 + i) * DM + lane * 8);
    }
    __builtin_amdgcn_sched_barrier(0);
    __builtin_amdgcn_s_barrier();
    __builtin_amdgcn_sched_barrier(0);
    {
      float xf[8];
      if (lane >= 56) xv = u32x4{0, 0, 0, 0};
#pragma unroll
      for (int k = 0; k < 4; ++k) { xf[2 * k] = __uint_as_float(xv[k] << 16); xf[2 * k + 1] = __uint_as_float(xv[k] & 0xffff0000u); }
      float s = 0.f;
#pragma unroll
      for (int k = 0; k < 8; ++k) s += xf[k];
      float mean = wave_sum(s) * (1.0f / DM);
      float v = 0.f;
      if (lane < 56) {
#pragma unroll
        for (int k = 0; k < 8; ++k) { float d = xf[k] - mean; v = fmaf(d, d, v); }
      }
      float var = wave_sum(v) * (1.0f / DM);
      float mb = rbf(mean), vb = rbf(var);
      float den = rbf(sqrtf(rbf(vb + 1e-5f)));
      if (lane < 56) {
#pragma unroll
        for (int k = 0; k < 8; ++k) L.xn[lane * 8 + k] = rbf(rbf(xf[k] - mb) / den);
      }
      asm volatile("s_waitcnt lgkmcnt(0)" ::: "memory");
    }
    bar();
    bar();
    {
      float v = 0.f, g = 0.f, u = 0.f;
#pragma unroll
      for (int w = 0; w < 8; ++w) { v += L.qred[w][lane]; g += L.gred[w][lane]; u += L.gred[w][64 + lane]; }
      gstore(G + G_Q + (size_t)b * 64 + lane, v);
      if (hasF) { gstore(G + G_GU + (size_t)b * 128 + lane, g); gstore(G + G_GU + (size_t)b * 128 + 64 + lane, u); }
    }
    {
      unsigned spins = 0;
      float qv[8];
      for (;;) {
        u64 g = gload(G + G_Q + (size_t)(kvh * 32 + (lane & 31)) * 64 + 63);
        if (__all((g >> 32) == TAG)) break;
        if (++spins > SPIN_CAP) break;
        __builtin_amdgcn_s_sleep(1);
      }
      for (;;) {
        bool ok = true;
        u64 gq[32];
#pragma unroll
        for (int j = 0; j < 32; ++j) gq[j] = gload(G + G_Q + (size_t)(kvh * 32 + j) * 64 + lane);
#pragma unroll
        for (int j = 0; j < 8; ++j) {
          float s = 0.f;
#pragma unroll
          for (int kq = 0; kq < 4; ++kq) {
            u64 g = gq[j * 4 + kq];
            s += __uint_as_float((unsigned)g); ok &= (g >> 32) == TAG;
          }
          qv[j] = s;
        }
        if (__all(ok)) break;
        if (++spins > SPIN_CAP) break;
      }
#pragma unroll
      for (int j = 0; j < 8; ++j) {
        float nb = dpp<0xB1>(qv[j]);
        if ((lane & 1) == 0) L.qs[j * 32 + (lane >> 1)] = cvtpk(qv[j], nb);
      }
    }
    bar();
    if (hasF) {
      unsigned spins = 0;
      float s = 0.f;
      for (;;) {
        bool ok = true; s = 0.f;
#pragma unroll
        for (int p = 0; p < 2; ++p) {
          const int kq = p * 4 + (lane >> 4), vi = lane & 15;
          u64 g = gload(G + G_GU + (size_t)((b >> 3) * 8 + kq) * 128 + (vi >> 3) * 64 + (b & 7) * 8 + (vi & 7));
          s += __uint_as_float((unsigned)g); ok &= (g >> 32) == TAG;
        }
        if (__all(ok)) break;
        if (++spins > SPIN_CAP) break;
      }
      s = xsum16_32(s);
      float g = rbf(s), u = rbf(__shfl(s, (lane & 7) + 8, 64));
      float hv = rbf(rbf(1.0f / (1.0f + expf(-g))) * u);
      float acc[8] = {0, 0, 0, 0, 0, 0, 0, 0};
#pragma unroll
      for (int i = 0; i < 8; ++i) fma8(acc, wdv[i], __shfl(hv, i, 64));
      if (lane < 56) {
#pragma unroll
        for (int k = 0; k < 8; ++k) gstore(G + G_FR + (size_t)b * DM + lane * 8 + k, acc[k]);
      }
    }
    bar();
    bar();
    bar();
    if (!hasF) return;
    bar();
    bar();
    bar();
    if (rg != 0) return;
    bar();
  }
}

extern "C" void kernel_launch(void* const* d_in, const int* in_sizes, int n_in,
                              void* d_out, int out_size, void* d_ws, size_t ws_size,
                              hipStream_t stream) {
  fused_kernel<<<NWG, NTH, 0, stream>>>((const u16*)d_in[0], (const u16*)d_in[1], (const u16*)d_in[2],
                                        (const u16*)d_in[3], (const u16*)d_in[4], (const u16*)d_in[6],
                                        (const u16*)d_in[7], (const u16*)d_in[8], (u16*)d_out);
}
```

```cpp
#include <hip/hip_runtime.h>
#include <hip/hip_bf16.h>
#include <stdint.h>

typedef unsigned short u16;
typedef unsigned long long u64;
typedef __attribute__((ext_vector_type(4))) unsigned int u32x4;
typedef __attribute__((ext_vector_type(4))) float f32x4;

constexpr int DM = 448, DH = 256, SK = 65536, WIN = 4096, FF = 1792;
constexpr int NCHUNK = 32, CHUNK = 128;
#ifndef NV0
#define NV0 2
#endif
constexpr int NWG = 256, NTH = 576;
constexpr unsigned SPIN_CAP = 1u << 18;

constexpr size_t G_Q   = 0;
constexpr size_t G_GU  = G_Q + 64 * 4 * 64;
constexpr size_t G_ML  = G_GU + 28 * 8 * 128;
constexpr size_t G_PO  = G_ML + 256 * 4;
constexpr size_t G_FR  = G_PO + 256 * 512;
constexpr size_t G_AP  = G_FR + 224 * 448;
constexpr size_t G_FP  = G_AP + 32 * 448;
constexpr size_t G_END = G_FP + 32 * 448;

__device__ u64 G[G_END];
__device__ unsigned g_epoch[64];

__device__ __forceinline__ float bf2f(u16 v) { return __uint_as_float(((unsigned)v) << 16); }
__device__ __forceinline__ u16 f2bf(float f) {
  unsigned u = __float_as_uint(f);
  if ((u & 0x7fffffffu) > 0x7f800000u) return (u16)((u >> 16) | 0x40);
  u += 0x7fffu + ((u >> 16) & 1u);
  return (u16)(u >> 16);
}
__device__ __forceinline__ unsigned cvtpk(float lo, float hi) {
  unsigned r; asm("v_cvt_pk_bf16_f32 %0, %1, %2" : "=v"(r) : "v"(lo), "v"(hi)); return r;
}
__device__ __forceinline__ float rbf(float f) { return __uint_as_float(cvtpk(f, f) << 16); }
typedef __attribute__((ext_vector_type(2))) __bf16 bf16x2;
typedef __attribute__((ext_vector_type(2))) float f32x2;
__device__ __forceinline__ float dot2(unsigned a, unsigned b, float acc) {
  return __builtin_amdgcn_fdot2_f32_bf16(__builtin_bit_cast(bf16x2, a), __builtin_bit_cast(bf16x2, b), acc, false);
}

template <int CTRL> __device__ __forceinline__ float dpp(float x) {
  return __builtin_bit_cast(float, __builtin_amdgcn_update_dpp(0, __builtin_bit_cast(int, x), CTRL, 0xf, 0xf, true));
}
__device__ __forceinline__ float xsum16_32(float x) {
  auto s = __builtin_amdgcn_permlane16_swap(__float_as_uint(x), __float_as_uint(x), false, false);
  x = __uint_as_float(s[0]) + __uint_as_float(s[1]);
  auto t = __builtin_amdgcn_permlane32_swap(__float_as_uint(x), __float_as_uint(x), false, false);
  return __uint_as_float(t[0]) + __uint_as_float(t[1]);
}
__device__ __forceinline__ float xmax16_32(float x) {
  auto s = __builtin_amdgcn_permlane16_swap(__float_as_uint(x), __float_as_uint(x), false, false);
  x = fmaxf(__uint_as_float(s[0]), __uint_as_float(s[1]));
  auto t = __builtin_amdgcn_permlane32_swap(__float_as_uint(x), __float_as_uint(x), false, false);
  return fmaxf(__uint_as_float(t[0]), __uint_as_float(t[1]));
}
__device__ __forceinline__ float wave_sum(float v) {
  v += dpp<0xB1>(v); v += dpp<0x4E>(v); v += dpp<0x141>(v); v += dpp<0x140>(v);
  return xsum16_32(v);
}
__device__ __forceinline__ float wave_max(float v) {
  v = fmaxf(v, dpp<0xB1>(v)); v = fmaxf(v, dpp<0x4E>(v)); v = fmaxf(v, dpp<0x141>(v)); v = fmaxf(v, dpp<0x140>(v));
  return xmax16_32(v);
}

#define gstore(p, v) __hip_atomic_store((p), (TAG << 32) | (u64)__float_as_uint(v), __ATOMIC_RELAXED, __HIP_MEMORY_SCOPE_AGENT)
__device__ __forceinline__ u64 gload(const u64* p) {
  return __hip_atomic_load(p, __ATOMIC_RELAXED, __HIP_MEMORY_SCOPE_AGENT);
}
__device__ __forceinline__ void bar() {
  asm volatile("s_waitcnt lgkmcnt(0)" ::: "memory");
  __builtin_amdgcn_s_barrier();
  asm volatile("" ::: "memory");
}
__device__ __forceinline__ void fma8(float (&acc)[8], u32x4 w, float s) {
#pragma unroll
  for (int k = 0; k < 4; ++k) {
    acc[2 * k]     = fmaf(s, __uint_as_float(w[k] << 16), acc[2 * k]);
    acc[2 * k + 1] = fmaf(s, __uint_as_float(w[k] & 0xffff0000u), acc[2 * k + 1]);
  }
}
__device__ __forceinline__ u32x4 ldnt(const u16* p) { return __builtin_nontemporal_load((const u32x4*)p); }

struct LdsT {
  float xn[DM];
  float qred[8][64];
  float gred[8][128];
  unsigned qs[256];
  float sc[2][CHUNK];
  float ored[8][2][DH];
};

__global__ void __launch_bounds__(NTH, 1)
fused_kernel(const u16* __restrict__ x, const u16* __restrict__ kc, const u16* __restrict__ vc,
             const u16* __restrict__ wq, const u16* __restrict__ wo, const u16* __restrict__ wg,
             const u16* __restrict__ wu, const u16* __restrict__ wd,
             u16* __restrict__ out) {
  __shared__ LdsT L;
  const unsigned epoch = __hip_atomic_load(&g_epoch[0], __ATOMIC_RELAXED, __HIP_MEMORY_SCOPE_AGENT);
  const u64 TAG = (u64)(epoch + 1u);
  const int t = threadIdx.x, b = blockIdx.x, lane = t & 63, wave = t >> 6;
  const int kvh = b >> 5, ch = b & 31;
  const bool hasF = b < 224;
  const int cg = b % 7, rg = b / 7;

  if (wave < 8) {

    const int r8 = t >> 3;
    const int qrow = (b & 3) * 112 + r8, qcol = (b >> 2) * 64 + (t & 7) * 8;
    const int qrow1 = (r8 < 48) ? qrow + 64 : qrow;
    u32x4 wq0 = ldnt(wq + (size_t)qrow * 4096 + qcol);
    u32x4 wq1 = ldnt(wq + (size_t)qrow1 * 4096 + qcol);
    const int frow = (b & 7) * 56 + min(r8, 55), fcol = (hasF ? (b >> 3) * 64 : 0) + (t & 7) * 8;
    u32x4 wgv = ldnt(wg + (size_t)frow * FF + fcol), wuv = ldnt(wu + (size_t)frow * FF + fcol);
    const int half = lane >> 5, d8 = (lane & 31) * 8;
    const size_t base = ((size_t)kvh * SK + (SK - WIN) + (size_t)ch * CHUNK + wave * 16 + half) * DH + d8;
    u32x4 K[8], V[8];
    __builtin_amdgcn_sched_barrier(0);
    __builtin_amdgcn_s_barrier();
    __builtin_amdgcn_sched_barrier(0);
#pragma unroll
    for (int i = 0; i < 8; ++i) K[i] = ldnt(kc + base + (size_t)i * 2 * DH);
#pragma unroll
    for (int i = 0; i < NV0; ++i) V[i] = ldnt(vc + base + (size_t)i * 2 * DH);
    __builtin_amdgcn_sched_barrier(0);
    bar();
    {
      float acc[8] = {0, 0, 0, 0, 0, 0, 0, 0};
      fma8(acc, wq0, L.xn[qrow]);
      fma8(acc, wq1, (r8 < 48) ? L.xn[qrow1] : 0.f);
      float ag[8] = {0, 0, 0, 0, 0, 0, 0, 0}, au[8] = {0, 0, 0, 0, 0, 0, 0, 0};
      { float s = (r8 < 56) ? L.xn[frow] : 0.f; fma8(ag, wgv, s); fma8(au, wuv, s); }
#pragma unroll
      for (int k = 0; k < 8; ++k) {
        acc[k] = xsum16_32(acc[k] + dpp<0x128>(acc[k]));
        ag[k] = xsum16_32(ag[k] + dpp<0x128>(ag[k]));
        au[k] = xsum16_32(au[k] + dpp<0x128>(au[k]));
      }
      if (lane < 8) {
#pragma unroll
        for (int k = 0; k < 8; ++k) {
          L.qred[wave][lane * 8 + k] = acc[k];
          L.gred[wave][lane * 8 + k] = ag[k]; L.gred[wave][64 + lane * 8 + k] = au[k];
        }
      }
    }
    bar();
    bar();
    __builtin_amdgcn_sched_barrier(0);
#pragma unroll
    for (int i = NV0; i < 8; ++i) V[i] = ldnt(vc + base + (size_t)i * 2 * DH);
    u32x4 wov[2] = {{0, 0, 0, 0}, {0, 0, 0, 0}};
    if (hasF) {
      const u16* wp = wo + (size_t)(rg * 128 + (t >> 3)) * DM + cg * 64 + (t & 7) * 8;
      wov[0] = ldnt(wp); wov[1] = ldnt(wp + (size_t)64 * DM);
    }
    __builtin_amdgcn_sched_barrier(0);
    const u32x4 q0 = *(const u32x4*)&L.qs[d8 >> 1], q1 = *(const u32x4*)&L.qs[128 + (d8 >> 1)];
    float sv[16];
#pragma unroll
    for (int i = 0; i < 8; ++i) {
      float s0 = dot2(K[i][0], q0[0], 0.f), s1 = dot2(K[i][0], q1[0], 0.f);
      float t0 = dot2(K[i][1], q0[1], 0.f), t1 = dot2(K[i][1], q1[1], 0.f);
      s0 = dot2(K[i][2], q0[2], s0); s1 = dot2(K[i][2], q1[2], s1);
      t0 = dot2(K[i][3], q0[3], t0); t1 = dot2(K[i][3], q1[3], t1);
      sv[2 * i] = s0 + t0; sv[2 * i + 1] = s1 + t1;
    }
#pragma unroll
    for (int st = 0; st < 4; ++st) {
      const int n = 8 >> st, bit = 16 >> st;
#pragma unroll
      for (int j = 0; j < n; ++j) {
        float lo = sv[j], hi = sv[j + n];
        float keep = (lane & bit) ? hi : lo, give = (lane & bit) ? lo : hi;
        sv[j] = keep + __shfl_xor(give, bit, 64);
      }
    }
    {
      float s = sv[0] + __shfl_xor(sv[0], 1, 64);
      int vi = ((lane >> 4) & 1) * 8 + ((lane >> 3) & 1) * 4 + ((lane >> 2) & 1) * 2 + ((lane >> 1) & 1);
      int row = wave * 16 + (vi >> 1) * 2 + half, hh = vi & 1;
      if ((lane & 1) == 0) L.sc[hh][row] = rbf(s) * 0.0625f;
    }
    bar();
    float m0, m1, l0, l1;
    {
      float a0 = L.sc[0][lane], a1 = L.sc[0][lane + 64], c0 = L.sc[1][lane], c1 = L.sc[1][lane + 64];
      m0 = wave_max(fmaxf(a0, a1)); m1 = wave_max(fmaxf(c0, c1));
      l0 = wave_sum(__expf(a0 - m0) + __expf(a1 - m0));
      l1 = wave_sum(__expf(c0 - m1) + __expf(c1 - m1));
    }
    f32x2 A0[4], A1[4];
#pragma unroll
    for (int k = 0; k < 4; ++k) { A0[k] = f32x2{0.f, 0.f}; A1[k] = f32x2{0.f, 0.f}; }
#pragma unroll
    for (int i = 0; i < 8; ++i) {
      int row = wave * 16 + 2 * i + half;
      float p0 = __expf(L.sc[0][row] - m0), p1 = __expf(L.sc[1][row] - m1);
      const f32x2 P0 = {p0, p0}, P1 = {p1, p1};
#pragma unroll
      for (int k = 0; k < 4; ++k) {
        f32x2 v = {__uint_as_float(V[i][k] << 16), __uint_as_float(V[i][k] & 0xffff0000u)};
        A0[k] = __builtin_elementwise_fma(v, P0, A0[k]);
        A1[k] = __builtin_elementwise_fma(v, P1, A1[k]);
      }
    }
    float a0[8], a1[8];
#pragma unroll
    for (int k = 0; k < 4; ++k) { a0[2 * k] = A0[k][0]; a0[2 * k + 1] = A0[k][1]; a1[2 * k] = A1[k][0]; a1[2 * k + 1] = A1[k][1]; }
#pragma unroll
    for (int k = 0; k < 8; ++k) {
      auto tt = __builtin_amdgcn_permlane32_swap(__float_as_uint(a0[k]), __float_as_uint(a1[k]), false, false);
      a0[k] = __uint_as_float(tt[0]) + __uint_as_float(tt[1]);
    }
    {
      float* dst = &L.ored[wave][half][d8];
      *(f32x4*)dst = f32x4{a0[0], a0[1], a0[2], a0[3]};
      *(f32x4*)(dst + 4) = f32x4{a0[4], a0[5], a0[6], a0[7]};
    }
    bar();
    {
      const float* o = &L.ored[0][0][0];
      float s = 0.f;
#pragma unroll
      for (int w = 0; w < 8; ++w) s += o[w * 512 + t];
      gstore(G + G_PO + (size_t)b * 512 + t, s);
      if (t < 4) gstore(G + G_ML + b * 4 + t, t == 0 ? m0 : t == 1 ? l0 : t == 2 ? m1 : l1);
    }
    bar();
    if (!hasF) return;
    const int h = rg >> 1, kv3 = h >> 1, hh = h & 1, d0 = (rg & 1) * 128;
    float wgt8[8];
    {
      const int d = t & 127, cq = t >> 7;
      float ov[8], mm = 0.f, ll = 0.f;
      unsigned spins = 0;
      for (;;) {
        bool ok = true;
        u64 gm = (TAG << 32), gl = (TAG << 32);
        if (lane < 32) { gm = gload(G + G_ML + (kv3 * 32 + lane) * 4 + hh * 2); gl = gload(G + G_ML + (kv3 * 32 + lane) * 4 + hh * 2 + 1); }
#pragma unroll
        for (int j = 0; j < 8; ++j) {
          u64 g = gload(G + G_PO + (size_t)(kv3 * 32 + cq * 8 + j) * 512 + hh * 256 + d0 + d);
          ov[j] = __uint_as_float((unsigned)g); ok &= (g >> 32) == TAG;
        }
        ok &= ((gm >> 32) == TAG) && ((gl >> 32) == TAG);
        mm = __uint_as_float((unsigned)gm); ll = __uint_as_float((unsigned)gl);
        if (__all(ok)) break;
        if (++spins > SPIN_CAP) break;
      }
      if (lane >= 32) { mm = -INFINITY; ll = 0.f; }
      float M = wave_max(mm);
      float e = (lane < 32) ? __expf(mm - M) : 0.f;
      float Lsum = wave_sum(e * ll);
      float wv = e / Lsum;
      float s = 0.f;
#pragma unroll
      for (int j = 0; j < 8; ++j) s = fmaf(__shfl(wv, cq * 8 + j, 64), ov[j], s);
      float* fr = &L.ored[0][0][0];
      fr[t] = s;
      (void)wgt8;
    }
    bar();
    {
      const float* fr = &L.ored[0][0][0];
      const int r = t >> 3;
      float o0 = rbf(fr[r] + fr[128 + r] + fr[256 + r] + fr[384 + r]);
      float o1 = rbf(fr[64 + r] + fr[192 + r] + fr[320 + r] + fr[448 + r]);
      float acc[8] = {0, 0, 0, 0, 0, 0, 0, 0};
      fma8(acc, wov[0], o0); fma8(acc, wov[1], o1);
#pragma unroll
      for (int k = 0; k < 8; ++k) {
        float v = acc[k];
        v += __shfl_xor(v, 8, 64); v += __shfl_xor(v, 16, 64); v += __shfl_xor(v, 32, 64);
        acc[k] = v;
      }
      float* red = &L.ored[2][0][0];
      if (lane < 8) {
#pragma unroll
        for (int k = 0; k < 8; ++k) red[wave * 64 + lane * 8 + k] = acc[k];
      }
    }
    bar();
    if (wave == 0) {
      const float* red = &L.ored[2][0][0];
      float a = 0.f;
#pragma unroll
      for (int w = 0; w < 8; ++w) a += red[w * 64 + lane];
      float f = 0.f;
      unsigned spins = 0;
      for (;;) {
        bool ok = true; f = 0.f;
#pragma unroll
        for (int j = 0; j < 7; ++j) {
          u64 g = gload(G + G_FR + (size_t)(rg * 7 + j) * DM + cg * 64 + lane);
          f += __uint_as_float((unsigned)g); ok &= (g >> 32) == TAG;
        }
        if (__all(ok)) break;
        if (++spins > SPIN_CAP) break;
      }
      gstore(G + G_AP + rg * DM + cg * 64 + lane, a);
      gstore(G + G_FP + rg * DM + cg * 64 + lane, f);
    }
    if (rg != 0) return;
    {
      float a = 0.f, f = 0.f;
      unsigned spins = 0;
      for (;;) {
        bool ok = true; a = 0.f; f = 0.f;
#pragma unroll
        for (int j = 0; j < 4; ++j) {
          u64 ga = gload(G + G_AP + (wave * 4 + j) * DM + cg * 64 + lane);
          u64 gf = gload(G + G_FP + (wave * 4 + j) * DM + cg * 64 + lane);
          a += __uint_as_float((unsigned)ga); f += __uint_as_float((unsigned)gf);
          ok &= ((ga >> 32) == TAG) && ((gf >> 32) == TAG);
        }
        if (__all(ok)) break;
        if (++spins > SPIN_CAP) break;
      }
      float* red = &L.ored[4][0][0];
      red[wave * 128 + lane] = a; red[wave * 128 + 64 + lane] = f;
    }
    bar();
    if (wave == 0) {
      const float* red = &L.ored[4][0][0];
      float a = 0.f, f = 0.f;
#pragma unroll
      for (int w = 0; w < 8; ++w) { a += red[w * 128 + lane]; f += red[w * 128 + 64 + lane]; }
      const int c = cg * 64 + lane;
      float xo = bf2f(x[c]);
      out[c] = f2bf(rbf(xo + rbf(a)) + rbf(f));
      if (b == 0 && lane == 0) __hip_atomic_store(&g_epoch[0], epoch + 1u, __ATOMIC_RELAXED, __HIP_MEMORY_SCOPE_AGENT);
    }
  } else {
    u32x4 xv = *(const u32x4*)(x + min(lane, 55) * 8);
    __builtin_amdgcn_sched_barrier(0);
    u32x4 wdv[8];
#pragma unroll
    for (int i = 0; i < 8; ++i) wdv[i] = u32x4{0, 0, 0, 0};
    if (hasF && lane < 56) {
#pragma unroll
      for (int i = 0; i < 8; ++i) wdv[i] = ldnt(wd + (size_t)(b * 8 + i) * DM + lane * 8);
    }
    __builtin_amdgcn_sched_barrier(0);
    __builtin_amdgcn_s_barrier();
    __builtin_amdgcn_sched_barrier(0);
    {
      float xf[8];
      if (lane >= 56) xv = u32x4{0, 0, 0, 0};
#pragma unroll
      for (int k = 0; k < 4; ++k) { xf[2 * k] = __uint_as_float(xv[k] << 16); xf[2 * k + 1] = __uint_as_float(xv[k] & 0xffff0000u); }
      float s = 0.f;
#pragma unroll
      for (int k = 0; k < 8; ++k) s += xf[k];
      float mean = wave_sum(s) * (1.0f / DM);
      float v = 0.f;
      if (lane < 56) {
#pragma unroll
        for (int k = 0; k < 8; ++k) { float d = xf[k] - mean; v = fmaf(d, d, v); }
      }
      float var = wave_sum(v) * (1.0f / DM);
      float mb = rbf(mean), vb = rbf(var);
      float den = rbf(sqrtf(rbf(vb + 1e-5f)));
      if (lane < 56) {
#pragma unroll
        for (int k = 0; k < 8; ++k) L.xn[lane * 8 + k] = rbf(rbf(xf[k] - mb) / den);
      }
      asm volatile("s_waitcnt lgkmcnt(0)" ::: "memory");
    }
    bar();
    bar();
    {
      float v = 0.f, g = 0.f, u = 0.f;
#pragma unroll
      for (int w = 0; w < 8; ++w) { v += L.qred[w][lane]; g += L.gred[w][lane]; u += L.gred[w][64 + lane]; }
      gstore(G + G_Q + (size_t)b * 64 + lane, v);
      if (hasF) { gstore(G + G_GU + (size_t)b * 128 + lane, g); gstore(G + G_GU + (size_t)b * 128 + 64 + lane, u); }
    }
    {
      unsigned spins = 0;
      float qv[8];
      for (;;) {
        bool ok = true;
        u64 gq[32];
#pragma unroll
        for (int j = 0; j < 32; ++j) gq[j] = gload(G + G_Q + (size_t)(kvh * 32 + j) * 64 + lane);
#pragma unroll
        for (int j = 0; j < 8; ++j) {
          float s = 0.f;
#pragma unroll
          for (int kq = 0; kq < 4; ++kq) {
            u64 g = gq[j * 4 + kq];
            s += __uint_as_float((unsigned)g); ok &= (g >> 32) == TAG;
          }
          qv[j] = s;
        }
        if (__all(ok)) break;
        if (++spins > SPIN_CAP) break;
      }
#pragma unroll
      for (int j = 0; j < 8; ++j) {
        float nb = dpp<0xB1>(qv[j]);
        if ((lane & 1) == 0) L.qs[j * 32 + (lane >> 1)] = cvtpk(qv[j], nb);
      }
    }
    bar();
    if (hasF) {
      unsigned spins = 0;
      float s = 0.f;
      for (;;) {
        bool ok = true; s = 0.f;
#pragma unroll
        for (int p = 0; p < 2; ++p) {
          const int kq = p * 4 + (lane >> 4), vi = lane & 15;
          u64 g = gload(G + G_GU + (size_t)((b >> 3) * 8 + kq) * 128 + (vi >> 3) * 64 + (b & 7) * 8 + (vi & 7));
          s += __uint_as_float((unsigned)g); ok &= (g >> 32) == TAG;
        }
        if (__all(ok)) break;
        if (++spins > SPIN_CAP) break;
      }
      s = xsum16_32(s);
      float g = rbf(s), u = rbf(__shfl(s, (lane & 7) + 8, 64));
      float hv = rbf(rbf(1.0f / (1.0f + expf(-g))) * u);
      float acc[8] = {0, 0, 0, 0, 0, 0, 0, 0};
#pragma unroll
      for (int i = 0; i < 8; ++i) fma8(acc, wdv[i], __shfl(hv, i, 64));
      if (lane < 56) {
#pragma unroll
        for (int k = 0; k < 8; ++k) gstore(G + G_FR + (size_t)b * DM + lane * 8 + k, acc[k]);
      }
    }
    bar();
    bar();
    bar();
    if (!hasF) return;
    bar();
    bar();
    if (rg != 0) return;
    bar();
  }
}

extern "C" void kernel_launch(void* const* d_in, const int* in_sizes, int n_in,
                              void* d_out, int out_size, void* d_ws, size_t ws_size,
                              hipStream_t stream) {
  fused_kernel<<<NWG, NTH, 0, stream>>>((const u16*)d_in[0], (const u16*)d_in[1], (const u16*)d_in[2],
                                        (const u16*)d_in[3], (const u16*)d_in[4], (const u16*)d_in[6],
                                        (const u16*)d_in[7], (const u16*)d_in[8], (u16*)d_out);
}
```

```cpp
#include <hip/hip_runtime.h>
#include <hip/hip_bf16.h>
#include <stdint.h>

typedef unsigned short u16;
typedef __attribute__((ext_vector_type(4))) unsigned int u32x4;
typedef __attribute__((ext_vector_type(4))) float f32x4;
typedef __attribute__((ext_vector_type(2))) __bf16 bf16x2;
typedef __attribute__((ext_vector_type(2))) float f32x2;

constexpr int DM = 448, DH = 256, SK = 65536, WIN = 4096, FF = 1792;
constexpr int CHUNK = 128;
constexpr int NWG = 256, NTH = 576, NV0 = 2;
constexpr unsigned SPIN_CAP = 1u << 18;

constexpr unsigned G_Q   = 16;
constexpr unsigned G_GU  = G_Q + 256 * 32;
constexpr unsigned G_ML  = G_GU + 224 * 64;
constexpr unsigned G_PO  = G_ML + 256 * 2;
constexpr unsigned G_FR  = G_PO + 256 * 256;
constexpr unsigned G_AF  = G_FR + 224 * 224;
constexpr unsigned G_END = G_AF + 32 * 448;

__device__ __forceinline__ float bf2f(u16 v) { return __uint_as_float(((unsigned)v) << 16); }
__device__ __forceinline__ unsigned cvtpk(float lo, float hi) {
  unsigned r; asm("v_cvt_pk_bf16_f32 %0, %1, %2" : "=v"(r) : "v"(lo), "v"(hi)); return r;
}
__device__ __forceinline__ float rbf(float f) { return __uint_as_float(cvtpk(f, f) << 16); }
__device__ __forceinline__ float dot2(unsigned a, unsigned b, float acc) {
  return __builtin_amdgcn_fdot2_f32_bf16(__builtin_bit_cast(bf16x2, a), __builtin_bit_cast(bf16x2, b), acc, false);
}
template <int CTRL> __device__ __forceinline__ float dpp(float x) {
  return __builtin_bit_cast(float, __builtin_amdgcn_update_dpp(0, __builtin_bit_cast(int, x), CTRL, 0xf, 0xf, true));
}
__device__ __forceinline__ float xsum16_32(float x) {
  auto s = __builtin_amdgcn_permlane16_swap(__float_as_uint(x), __float_as_uint(x), false, false);
  x = __uint_as_float(s[0]) + __uint_as_float(s[1]);
  auto t = __builtin_amdgcn_permlane32_swap(__float_as_uint(x), __float_as_uint(x), false, false);
  return __uint_as_float(t[0]) + __uint_as_float(t[1]);
}
__device__ __forceinline__ float xmax16_32(float x) {
  auto s = __builtin_amdgcn_permlane16_swap(__float_as_uint(x), __float_as_uint(x), false, false);
  x = fmaxf(__uint_as_float(s[0]), __uint_as_float(s[1]));
  auto t = __builtin_amdgcn_permlane32_swap(__float_as_uint(x), __float_as_uint(x), false, false);
  return fmaxf(__uint_as_float(t[0]), __uint_as_float(t[1]));
}
__device__ __forceinline__ float wave_sum(float v) {
  v += dpp<0xB1>(v); v += dpp<0x4E>(v); v += dpp<0x141>(v); v += dpp<0x140>(v);
  return xsum16_32(v);
}
__device__ __forceinline__ float wave_max(float v) {
  v = fmaxf(v, dpp<0xB1>(v)); v = fmaxf(v, dpp<0x4E>(v)); v = fmaxf(v, dpp<0x141>(v)); v = fmaxf(v, dpp<0x140>(v));
  return xmax16_32(v);
}
__device__ __forceinline__ void bar() {
  asm volatile("s_waitcnt lgkmcnt(0)" ::: "memory");
  __builtin_amdgcn_s_barrier();
  asm volatile("" ::: "memory");
}
__device__ __forceinline__ void fma8(float (&acc)[8], u32x4 w, float s) {
#pragma unroll
  for (int k = 0; k < 4; ++k) {
    acc[2 * k]     = fmaf(s, __uint_as_float(w[k] << 16), acc[2 * k]);
    acc[2 * k + 1] = fmaf(s, __uint_as_float(w[k] & 0xffff0000u), acc[2 * k + 1]);
  }
}
__device__ __forceinline__ u32x4 ldnt(const u16* p) { return __builtin_nontemporal_load((const u32x4*)p); }

#define GST(slot, a, b) __builtin_amdgcn_raw_buffer_store_b128(u32x4{T0, T1, __float_as_uint(a), __float_as_uint(b)}, rsrc, (slot) * 16u, 0, 16)
__device__ __forceinline__ unsigned opaque(unsigned v) { asm volatile("" : "+v"(v)); return v; }
#define GLD(slot) __builtin_amdgcn_raw_buffer_load_b128(rsrc, opaque((slot) * 16u), 0, 16)
#define GOK(g) ((g)[0] == T0 && (g)[1] == T1)
#define GA(g) __uint_as_float((g)[2])
#define GB(g) __uint_as_float((g)[3])
#define SPIN_END(ok) asm volatile("" ::: "memory"); if (__all(ok)) break; if (++spins > SPIN_CAP) break;

struct LdsT {
  float xn[DM];
  float qred[8][64];
  float gred[8][128];
  unsigned qs[256];
  float sc[2][CHUNK];
  float ored[8][2][DH];
};

__global__ void __launch_bounds__(NTH, 1)
fused_kernel(const u16* __restrict__ x, const u16* __restrict__ kc, const u16* __restrict__ vc,
             const u16* __restrict__ wq, const u16* __restrict__ wo, const u16* __restrict__ wg,
             const u16* __restrict__ wu, const u16* __restrict__ wd, unsigned* ws,
             u16* __restrict__ out) {
  __shared__ LdsT L;
  const __amdgpu_buffer_rsrc_t rsrc = __builtin_amdgcn_make_buffer_rsrc(ws, 0, G_END * 16u, 0x00020000);
  const unsigned epoch = __builtin_amdgcn_readfirstlane(__hip_atomic_load(ws, __ATOMIC_RELAXED, __HIP_MEMORY_SCOPE_AGENT));
  const unsigned T0 = epoch + 1u, T1 = (T0 * 0x9E3779B9u) ^ 0x5BD1E995u;
  const int t = threadIdx.x, b = blockIdx.x, lane = t & 63, wave = t >> 6;
  const int kvh = b >> 5, ch = b & 31;
  const bool hasF = b < 224;
  const int cg = b % 7, rg = b / 7;

  if (wave < 8) {
    const int r8 = t >> 3;
    const int qrow = (b & 3) * 112 + r8, qcol = (b >> 2) * 64 + (t & 7) * 8;
    const int qrow1 = (r8 < 48) ? qrow + 64 : qrow;
    u32x4 wq0 = ldnt(wq + (size_t)qrow * 4096 + qcol);
    u32x4 wq1 = ldnt(wq + (size_t)qrow1 * 4096 + qcol);
    const int frow = (b & 7) * 56 + min(r8, 55), fcol = (hasF ? (b >> 3) * 64 : 0) + (t & 7) * 8;
    u32x4 wgv = ldnt(wg + (size_t)frow * FF + fcol), wuv = ldnt(wu + (size_t)frow * FF + fcol);
    const int half = lane >> 5, d8 = (lane & 31) * 8;
    const size_t base = ((size_t)kvh * SK + (SK - WIN) + (size_t)ch * CHUNK + wave * 16 + half) * DH + d8;
    u32x4 K[8], V[8];
    __builtin_amdgcn_sched_barrier(0);
    __builtin_amdgcn_s_barrier();
    __builtin_amdgcn_sched_barrier(0);
#pragma unroll
    for (int i = 0; i < 8; ++i) K[i] = ldnt(kc + base + (size_t)i * 2 * DH);
#pragma unroll
    for (int i = 0; i < NV0; ++i) V[i] = ldnt(vc + base + (size_t)i * 2 * DH);
    __builtin_amdgcn_sched_barrier(0);
    bar();
    {
      float acc[8] = {0, 0, 0, 0, 0, 0, 0, 0};
      fma8(acc, wq0, L.xn[qrow]);
      fma8(acc, wq1, (r8 < 48) ? L.xn[qrow1] : 0.f);
      float ag[8] = {0, 0, 0, 0, 0, 0, 0, 0}, au[8] = {0, 0, 0, 0, 0, 0, 0, 0};
      { float s = (r8 < 56) ? L.xn[frow] : 0.f; fma8(ag, wgv, s); fma8(au, wuv, s); }
#pragma unroll
      for (int k = 0; k < 8; ++k) {
        acc[k] = xsum16_32(acc[k] + dpp<0x128>(acc[k]));
        ag[k] = xsum16_32(ag[k] + dpp<0x128>(ag[k]));
        au[k] = xsum16_32(au[k] + dpp<0x128>(au[k]));
      }
      if (lane < 8) {
#pragma unroll
        for (int k = 0; k < 8; ++k) {
          L.qred[wave][lane * 8 + k] = acc[k];
          L.gred[wave][lane * 8 + k] = ag[k]; L.gred[wave][64 + lane * 8 + k] = au[k];
        }
      }
    }
    bar();
    bar();
    __builtin_amdgcn_sched_barrier(0);
#pragma unroll
    for (int i = NV0; i < 8; ++i) V[i] = ldnt(vc + base + (size_t)i * 2 * DH);
    u32x4 wov[2] = {{0, 0, 0, 0}, {0, 0, 0, 0}};
    if (hasF) {
      const u16* wp = wo + (size_t)(rg * 128 + (t >> 3)) * DM + cg * 64 + (t & 7) * 8;
      wov[0] = ldnt(wp); wov[1] = ldnt(wp + (size_t)64 * DM);
    }
    __builtin_amdgcn_sched_barrier(0);
    const u32x4 q0 = *(const u32x4*)&L.qs[d8 >> 1], q1 = *(const u32x4*)&L.qs[128 + (d8 >> 1)];
    float sv[16];
#pragma unroll
    for (int i = 0; i < 8; ++i) {
      float s0 = dot2(K[i][0], q0[0], 0.f), s1 = dot2(K[i][0], q1[0], 0.f);
      float t0 = dot2(K[i][1], q0[1], 0.f), t1 = dot2(K[i][1], q1[1], 0.f);
      s0 = dot2(K[i][2], q0[2], s0); s1 = dot2(K[i][2], q1[2], s1);
      t0 = dot2(K[i][3], q0[3], t0); t1 = dot2(K[i][3], q1[3], t1);
      sv[2 * i] = s0 + t0; sv[2 * i + 1] = s1 + t1;
    }
#pragma unroll
    for (int st = 0; st < 4; ++st) {
      const int n = 8 >> st, bit = 16 >> st;
#pragma unroll
      for (int j = 0; j < n; ++j) {
        float lo = sv[j], hi = sv[j + n];
        float keep = (lane & bit) ? hi : lo, give = (lane & bit) ? lo : hi;
        sv[j] = keep + __shfl_xor(give, bit, 64);
      }
    }
    {
      float s = sv[0] + __shfl_xor(sv[0], 1, 64);
      int vi = ((lane >> 4) & 1) * 8 + ((lane >> 3) & 1) * 4 + ((lane >> 2) & 1) * 2 + ((lane >> 1) & 1);
      int row = wave * 16 + (vi >> 1) * 2 + half, hh = vi & 1;
      if ((lane & 1) == 0) L.sc[hh][row] = rbf(s) * 0.0625f;
    }
    bar();
    float m0, m1, l0, l1;
    {
      float a0 = L.sc[0][lane], a1 = L.sc[0][lane + 64], c0 = L.sc[1][lane], c1 = L.sc[1][lane + 64];
      m0 = wave_max(fmaxf(a0, a1)); m1 = wave_max(fmaxf(c0, c1));
      l0 = wave_sum(__expf(a0 - m0) + __expf(a1 - m0));
      l1 = wave_sum(__expf(c0 - m1) + __expf(c1 - m1));
    }
    f32x2 A0[4], A1[4];
#pragma unroll
    for (int k = 0; k < 4; ++k) { A0[k] = f32x2{0.f, 0.f}; A1[k] = f32x2{0.f, 0.f}; }
#pragma unroll
    for (int i = 0; i < 8; ++i) {
      int row = wave * 16 + 2 * i + half;
      float p0 = __expf(L.sc[0][row] - m0), p1 = __expf(L.sc[1][row] - m1);
      const f32x2 P0 = {p0, p0}, P1 = {p1, p1};
#pragma unroll
      for (int k = 0; k < 4; ++k) {
        f32x2 v = {__uint_as_float(V[i][k] << 16), __uint_as_float(V[i][k] & 0xffff0000u)};
        A0[k] = __builtin_elementwise_fma(v, P0, A0[k]);
        A1[k] = __builtin_elementwise_fma(v, P1, A1[k]);
      }
    }
    float a0[8];
#pragma unroll
    for (int k = 0; k < 8; ++k) {
      auto tt = __builtin_amdgcn_permlane32_swap(__float_as_uint(A0[k >> 1][k & 1]), __float_as_uint(A1[k >> 1][k & 1]), false, false);
      a0[k] = __uint_as_float(tt[0]) + __uint_as_float(tt[1]);
    }
    {
      float* dst = &L.ored[wave][half][d8];
      *(f32x4*)dst = f32x4{a0[0], a0[1], a0[2], a0[3]};
      *(f32x4*)(dst + 4) = f32x4{a0[4], a0[5], a0[6], a0[7]};
    }
    bar();
    if (t < 256) {
      const float* o = &L.ored[0][0][0];
      float s0 = 0.f, s1 = 0.f;
#pragma unroll
      for (int w = 0; w < 8; ++w) { f32x2 v = *(const f32x2*)&o[w * 512 + 2 * t]; s0 += v[0]; s1 += v[1]; }
      GST(G_PO + b * 256 + t, s0, s1);
    } else if (t < 258) {
      GST(G_ML + b * 2 + (t - 256), t == 256 ? m0 : m1, t == 256 ? l0 : l1);
    }
    bar();
    if (!hasF) return;
    const int h = rg >> 1, kv3 = h >> 1, hh = h & 1, d0 = (rg & 1) * 128;
    {
      const int dp = t & 63, cq = wave;
      float ov[8], mm = 0.f, ll = 0.f;
      unsigned spins = 0;
      for (;;) {
        u32x4 gml = GLD(G_ML + (kv3 * 32 + (lane & 31)) * 2 + hh);
        bool ok = GOK(gml);
#pragma unroll
        for (int j = 0; j < 4; ++j) {
          u32x4 g = GLD(G_PO + (kv3 * 32 + cq * 4 + j) * 256 + hh * 128 + (d0 >> 1) + dp);
          ov[2 * j] = GA(g); ov[2 * j + 1] = GB(g); ok = ok && GOK(g);
        }
        mm = GA(gml); ll = GB(gml);
        SPIN_END(ok)
      }
      if (lane >= 32) { mm = -INFINITY; ll = 0.f; }
      float M = wave_max(mm);
      float e = (lane < 32) ? __expf(mm - M) : 0.f;
      float Lsum = wave_sum(e * ll);
      float wv = e / Lsum;
      float s0 = 0.f, s1 = 0.f;
#pragma unroll
      for (int j = 0; j < 4; ++j) { float w = __shfl(wv, cq * 4 + j, 64); s0 = fmaf(w, ov[2 * j], s0); s1 = fmaf(w, ov[2 * j + 1], s1); }
      float* fr = &L.ored[0][0][0];
      *(f32x2*)&fr[cq * 128 + 2 * dp] = f32x2{s0, s1};
    }
    bar();
    {
      const float* fr = &L.ored[0][0][0];
      const int r = t >> 3;
      float o0 = 0.f, o1 = 0.f;
#pragma unroll
      for (int w = 0; w < 8; ++w) { o0 += fr[w * 128 + r]; o1 += fr[w * 128 + 64 + r]; }
      o0 = rbf(o0); o1 = rbf(o1);
      float acc[8] = {0, 0, 0, 0, 0, 0, 0, 0};
      fma8(acc, wov[0], o0); fma8(acc, wov[1], o1);
#pragma unroll
      for (int k = 0; k < 8; ++k) acc[k] = xsum16_32(acc[k] + dpp<0x128>(acc[k]));
      float* red = &L.ored[4][0][0];
      if (lane < 8) {
#pragma unroll
        for (int k = 0; k < 8; ++k) red[wave * 64 + lane * 8 + k] = acc[k];
      }
    }
    bar();
    if (wave == 0) {
      const float* red = &L.ored[4][0][0];
      float a = 0.f;
#pragma unroll
      for (int w = 0; w < 8; ++w) a += red[w * 64 + lane];
      float f0 = 0.f, f1 = 0.f;
      unsigned spins = 0;
      for (;;) {
        bool ok = true; f0 = 0.f; f1 = 0.f;
#pragma unroll
        for (int j = 0; j < 7; ++j) {
          u32x4 g = GLD(G_FR + (rg * 7 + j) * 224 + cg * 32 + (lane >> 1));
          f0 += GA(g); f1 += GB(g); ok = ok && GOK(g);
        }
        SPIN_END(ok)
      }
      GST(G_AF + rg * DM + cg * 64 + lane, a, (lane & 1) ? f1 : f0);
    }
    if (rg != 0) return;
    {
      float a = 0.f, f = 0.f;
      unsigned spins = 0;
      for (;;) {
        bool ok = true; a = 0.f; f = 0.f;
#pragma unroll
        for (int j = 0; j < 4; ++j) {
          u32x4 g = GLD(G_AF + (wave * 4 + j) * DM + cg * 64 + lane);
          a += GA(g); f += GB(g); ok = ok && GOK(g);
        }
        SPIN_END(ok)
      }
      float* red = &L.ored[6][0][0];
      red[wave * 128 + lane] = a; red[wave * 128 + 64 + lane] = f;
    }
    bar();
    if (wave == 0) {
      const float* red = &L.ored[6][0][0];
      float a = 0.f, f = 0.f;
#pragma unroll
      for (int w = 0; w < 8; ++w) { a += red[w * 128 + lane]; f += red[w * 128 + 64 + lane]; }
      const int c = cg * 64 + lane;
      float xo = bf2f(x[c]);
      out[c] = (u16)(cvtpk(rbf(xo + rbf(a)) + rbf(f), 0.f) & 0xffffu);
      if (b == 0 && lane == 0) __hip_atomic_store(ws, epoch + 1u, __ATOMIC_RELAXED, __HIP_MEMORY_SCOPE_AGENT);
    }
  } else {
    u32x4 xv = *(const u32x4*)(x + min(lane, 55) * 8);
    __builtin_amdgcn_sched_barrier(0);
    u32x4 wdv[8];
#pragma unroll
    for (int i = 0; i < 8; ++i) wdv[i] = u32x4{0, 0, 0, 0};
    if (hasF && lane < 56) {
#pragma unroll
      for (int i = 0; i < 8; ++i) wdv[i] = ldnt(wd + (size_t)(b * 8 + i) * DM + lane * 8);
    }
    __builtin_amdgcn_sched_barrier(0);
    __builtin_amdgcn_s_barrier();
    __builtin_amdgcn_sched_barrier(0);
    {
      float xf[8];
      if (lane >= 56) xv = u32x4{0, 0, 0, 0};
#pragma unroll
      for (int k = 0; k < 4; ++k) { xf[2 * k] = __uint_as_float(xv[k] << 16); xf[2 * k + 1] = __uint_as_float(xv[k] & 0xffff0000u); }
      float s = 0.f;
#pragma unroll
      for (int k = 0; k < 8; ++k) s += xf[k];
      float mean = wave_sum(s) * (1.0f / DM);
      float v = 0.f;
      if (lane < 56) {
#pragma unroll
        for (int k = 0; k < 8; ++k) { float d = xf[k] - mean; v = fmaf(d, d, v); }
      }
      float var = wave_sum(v) * (1.0f / DM);
      float mb = rbf(mean), vb = rbf(var);
      float den = rbf(sqrtf(rbf(vb + 1e-5f)));
      if (lane < 56) {
#pragma unroll
        for (int k = 0; k < 8; ++k) L.xn[lane * 8 + k] = rbf(rbf(xf[k] - mb) / den);
      }
    }
    bar();
    bar();
    {
      float v0 = 0.f, v1 = 0.f, g0 = 0.f, g1 = 0.f;
#pragma unroll
      for (int w = 0; w < 8; ++w) {
        f32x2 q = *(const f32x2*)&L.qred[w][2 * (lane & 31)], g = *(const f32x2*)&L.gred[w][2 * lane];
        v0 += q[0]; v1 += q[1]; g0 += g[0]; g1 += g[1];
      }
      if (lane < 32) GST(G_Q + b * 32 + lane, v0, v1);
      if (hasF) GST(G_GU + b * 64 + lane, g0, g1);
    }
    {
      unsigned spins = 0;
      float qa[8], qb[8];
      for (;;) {
        bool ok = true;
        u32x4 gq[16];
#pragma unroll
        for (int j = 0; j < 16; ++j) gq[j] = GLD(G_Q + (kvh * 32 + (j >> 1) * 4 + (lane >> 5) * 2 + (j & 1)) * 32 + (lane & 31));
#pragma unroll
        for (int j = 0; j < 8; ++j) {
          qa[j] = GA(gq[2 * j]) + GA(gq[2 * j + 1]); qb[j] = GB(gq[2 * j]) + GB(gq[2 * j + 1]);
          ok = ok && GOK(gq[2 * j]) && GOK(gq[2 * j + 1]);
        }
        SPIN_END(ok)
      }
#pragma unroll
      for (int j = 0; j < 8; ++j) {
        auto ta = __builtin_amdgcn_permlane32_swap(__float_as_uint(qa[j]), __float_as_uint(qa[j]), false, false);
        auto tb = __builtin_amdgcn_permlane32_swap(__float_as_uint(qb[j]), __float_as_uint(qb[j]), false, false);
        float a = __uint_as_float(ta[0]) + __uint_as_float(ta[1]), c = __uint_as_float(tb[0]) + __uint_as_float(tb[1]);
        if (lane < 32) L.qs[j * 32 + lane] = cvtpk(a, c);
      }
    }
    bar();
    if (hasF) {
      unsigned spins = 0;
      float s0, s1;
      for (;;) {
        u32x4 g = GLD(G_GU + ((b >> 3) * 8 + (lane >> 3)) * 64 + ((lane >> 2) & 1) * 32 + (b & 7) * 4 + (lane & 3));
        s0 = GA(g); s1 = GB(g);
        bool ok = GOK(g);
        SPIN_END(ok)
      }
      s0 = xsum16_32(s0 + dpp<0x128>(s0)); s1 = xsum16_32(s1 + dpp<0x128>(s1));
      float acc[8] = {0, 0, 0, 0, 0, 0, 0, 0};
#pragma unroll
      for (int i = 0; i < 8; ++i) {
        float g = rbf(__shfl((i & 1) ? s1 : s0, i >> 1, 64)), u = rbf(__shfl((i & 1) ? s1 : s0, 4 + (i >> 1), 64));
        float hv = rbf(rbf(1.0f / (1.0f + expf(-g))) * u);
        fma8(acc, wdv[i], hv);
      }
      if (lane < 56) {
#pragma unroll
        for (int k = 0; k < 4; ++k) GST(G_FR + b * 224 + lane * 4 + k, acc[2 * k], acc[2 * k + 1]);
      }
    }
    bar();
    bar();
    bar();
    if (!hasF) return;
    bar();
    bar();
    if (rg != 0) return;
    bar();
  }
}

extern "C" void kernel_launch(void* const* d_in, const int* in_sizes, int n_in,
                              void* d_out, int out_size, void* d_ws, size_t ws_size,
                              hipStream_t stream) {
  fused_kernel<<<NWG, NTH, 0, stream>>>((const u16*)d_in[0], (const u16*)d_in[1], (const u16*)d_in[2],
                                        (const u16*)d_in[3], (const u16*)d_in[4], (const u16*)d_in[6],
                                        (const u16*)d_in[7], (const u16*)d_in[8], (unsigned*)d_ws, (u16*)d_out);
}
```
